```python
import torch

def _softmax(x, axis=-1):
    return torch.softmax(x, dim=axis)

def _gelu(x):
    return torch.nn.functional.gelu(x, approximate='tanh')

def _silu(x):
    return torch.nn.functional.silu(x)

def _sigmoid(x):
    return torch.sigmoid(x)

def _layernorm(x, axis=-1, eps=1e-05):
    m = x.mean(dim=axis, keepdim=True)
    v = x.var(dim=axis, keepdim=True, unbiased=False)
    return (x - m) / torch.sqrt(v + eps)

def _scatter_add(tgt, idx, src, axis=0):
    return tgt.clone().index_add_(axis, idx, src)

def _segment_sum(src, seg, n):
    out = torch.zeros((n,) + src.shape[1:], dtype=src.dtype)
    return out.index_add_(0, seg, src)

def _pad(x, pw):
    flat = tuple((v for pair in reversed(pw) for v in pair))
    return torch.nn.functional.pad(x, flat)
kernel_name = 'adaLN_cond__linear_kernel_hedgehog__moe_arch_mod__prompt'

def setup_inputs():

    _w = lambda a: (lambda t: t.to(torch.bfloat16) if t.is_floating_point() else t)(torch.as_tensor(a))

    def _A_setup():
        B, S, D, H = (1, 2048, 1024, 32)
        Dh = D // H
        return {'x': _w((lambda *s: torch.randn(*s, dtype=torch.bfloat16) * 0.05)(B, S, D)), 'wqkv': _w((lambda *s: torch.randn(*s, dtype=torch.bfloat16) * 0.4)(D, 3 * D)), 'w_phi': _w((lambda *s: torch.randn(*s, dtype=torch.bfloat16) * 0.4)(H, Dh, Dh) * Dh ** (-0.5)), 'wo': _w((lambda *s: torch.randn(*s, dtype=torch.bfloat16) * 0.05)(D, D))}
    _A_in = _A_setup()

    def _B_setup():
        B, S, D, F, C = (1, 2048, 1024, 3584, 2)
        return {'x': _w((lambda *s: torch.randn(*s, dtype=torch.bfloat16) * 0.05)(B, S, D)), 'w_r': _w((lambda *s: torch.randn(*s, dtype=torch.bfloat16) * 0.1)(D)), 'w_gate': _w((lambda *s: torch.randn(*s, dtype=torch.bfloat16) * 0.05)(D, F)), 'w_up': _w((lambda *s: torch.randn(*s, dtype=torch.bfloat16) * 0.05)(D, F)), 'w_down': _w((lambda *s: torch.randn(*s, dtype=torch.bfloat16) * 0.05)(F, D))}
    _B_in = _B_setup()
    return {'A_x': _A_in['x'], 'A_wqkv': _A_in['wqkv'], 'A_w_phi': _A_in['w_phi'], 'A_wo': _A_in['wo'], 'B_x': _B_in['x'], 'B_w_r': _B_in['w_r'], 'B_w_gate': _B_in['w_gate'], 'B_w_up': _B_in['w_up'], 'B_w_down': _B_in['w_down'], 'c': _w((lambda *s: torch.randn(*s, dtype=torch.bfloat16) * 0.2)(1, 1024) * 8.0), 'w_mod': _w((lambda *s: torch.randn(*s, dtype=torch.bfloat16) * 0.2)(1024, 6 * 1024) * 1024 ** (-0.5) * 8.0)}

def reference(A_x, A_wqkv, A_w_phi, A_wo, B_x, B_w_r, B_w_gate, B_w_up, B_w_down, c, w_mod):

    def _A_ref(x, wqkv, w_phi, wo):
        B, S, D = x.shape
        H = 32
        Dh = D // H
        qkv = (lambda x, *d: x.permute(*d))((x @ wqkv).reshape(B, S, 3, H, Dh), 2, 0, 3, 1, 4)
        q, k, v = (qkv[0], qkv[1], qkv[2])
        pq = torch.einsum('bhsd,hdf->bhsf', q, w_phi)
        pk = torch.einsum('bhsd,hdf->bhsf', k, w_phi)
        phi_q = torch.concatenate([_softmax(pq, axis=-1), _softmax(-pq, axis=-1)], axis=-1)
        phi_k = torch.concatenate([_softmax(pk, axis=-1), _softmax(-pk, axis=-1)], axis=-1)
        kv = torch.einsum('bhsf,bhsd->bhfd', phi_k, v)
        z = torch.einsum('bhsf->bhf', phi_k)
        num = torch.einsum('bhqf,bhfd->bhqd', phi_q, kv)
        den = torch.einsum('bhqf,bhf->bhq', phi_q, z)[..., None] + 1e-06
        o = num / den
        return (lambda x, *d: x.permute(*d))(o, 0, 2, 1, 3).reshape(B, S, D) @ wo

    def _B_ref(x, w_r, w_gate, w_up, w_down):
        B, S, D = x.shape
        C = 2
        r = x @ w_r
        idx = torch.argsort(-r, axis=-1)[:, :C]
        idx_e = (lambda x, n, axis: torch.repeat_interleave(x, n, dim=axis))(idx.reshape(B, C, 1), D, axis=2)
        x_sel = (lambda x, i, axis: torch.take_along_dim(x, i, dim=axis))(x, idx_e, axis=1)
        g = _silu(x_sel @ w_gate)
        u = x_sel @ w_up
        y = g * u @ w_down
        r_sel = _sigmoid((lambda x, i, axis: torch.take_along_dim(x, i, dim=axis))(r, idx, axis=1))
        y = y * r_sel.reshape(B, C, 1)
        b_off = (torch.arange(B) * S).reshape(B, 1)
        flat_idx = (idx + b_off).reshape(B * C)
        zero = (x * 0.0).reshape(B * S, D)
        upd = _scatter_add(zero, flat_idx, y.reshape(B * C, D), axis=0)
        return x + upd.reshape(B, S, D)
    x = A_x
    mod = (_silu(c) @ w_mod).reshape(c.shape[0], 6, 1, -1)
    sa, ca, ga, sb, cb, gb = (mod[:, 0].clone(), mod[:, 1].clone(), mod[:, 2].clone(), mod[:, 3].clone(), mod[:, 4].clone(), mod[:, 5].clone())
    h = _layernorm(x, axis=-1) * (1 + ca) + sa
    x = x + ga * _A_ref(h, A_wqkv, A_w_phi, A_wo)
    h = _layernorm(x, axis=-1) * (1 + cb) + sb
    x = x + gb * _B_ref(h, B_w_r, B_w_gate, B_w_up, B_w_down)
    return x
```

```cpp
#include <hip/hip_runtime.h>
#include <hip/hip_bf16.h>
#include <stdint.h>

constexpr int S  = 2048;
constexpr int D  = 1024;
constexpr int H  = 32;
constexpr int F  = 3584;

typedef short  s16x4  __attribute__((ext_vector_type(4)));
typedef short  bf16x8 __attribute__((ext_vector_type(8)));
typedef float  f32x4  __attribute__((ext_vector_type(4)));
typedef unsigned short u16;
#define LDSP(T) __attribute__((address_space(3))) T*
#define DI __device__ __forceinline__

DI float bf2f(u16 x) { return __uint_as_float(((unsigned)x) << 16); }
DI u16 f2bf(float f) {
  __hip_bfloat16 b = __float2bfloat16(f);
  return *reinterpret_cast<u16*>(&b);
}
DI float rbf(float f) { return bf2f(f2bf(f)); }
DI unsigned pack2(float a, float b) { return (unsigned)f2bf(a) | ((unsigned)f2bf(b) << 16); }
DI float lo16(unsigned u) { return __uint_as_float(u << 16); }
DI float hi16(unsigned u) { return __uint_as_float(u & 0xffff0000u); }

__global__ __launch_bounds__(256) void k_mod(const u16* __restrict__ c, const u16* __restrict__ wmod,
                                             float* __restrict__ part) {
  __shared__ float red[32][65];
  const int cc = blockIdx.x % 96, ks = blockIdx.x / 96;
  const int t = threadIdx.x, cq = t & 7, rr = t >> 3;
  const int col0 = cc * 64 + cq * 8;
  uint4 w[8];
#pragma unroll
  for (int i = 0; i < 8; ++i) {
    const int k = ks * 256 + i * 32 + rr;
    w[i] = *reinterpret_cast<const uint4*>(wmod + (size_t)k * 6144 + col0);
  }
  float acc[8];
#pragma unroll
  for (int j = 0; j < 8; ++j) acc[j] = 0.f;
#pragma unroll
  for (int i = 0; i < 8; ++i) {
    const int k = ks * 256 + i * 32 + rr;
    const float cv = bf2f(c[k]);
    const float s = rbf(cv / (1.f + __expf(-cv)));
    acc[0] += s * lo16(w[i].x); acc[1] += s * hi16(w[i].x);
    acc[2] += s * lo16(w[i].y); acc[3] += s * hi16(w[i].y);
    acc[4] += s * lo16(w[i].z); acc[5] += s * hi16(w[i].z);
    acc[6] += s * lo16(w[i].w); acc[7] += s * hi16(w[i].w);
  }
#pragma unroll
  for (int j = 0; j < 8; ++j) red[rr][cq * 8 + j] = acc[j];
  __syncthreads();
  if (t < 64) {
    float s = 0.f;
#pragma unroll
    for (int i = 0; i < 32; ++i) s += red[i][t];
    part[ks * 6144 + cc * 64 + t] = s;
  }
}

__global__ __launch_bounds__(256) void k_ln1(const u16* __restrict__ x, const float* __restrict__ modpart,
                                             u16* __restrict__ h) {
  __shared__ float sc[D], sh[D];
  const int t = threadIdx.x;
  {
    const int c4 = t * 4;
    float4 a = *reinterpret_cast<const float4*>(modpart + c4);
    float4 b = *reinterpret_cast<const float4*>(modpart + D + c4);
#pragma unroll
    for (int p = 1; p < 4; ++p) {
      float4 a2 = *reinterpret_cast<const float4*>(modpart + p * 6144 + c4);
      float4 b2 = *reinterpret_cast<const float4*>(modpart + p * 6144 + D + c4);
      a.x += a2.x; a.y += a2.y; a.z += a2.z; a.w += a2.w;
      b.x += b2.x; b.y += b2.y; b.z += b2.z; b.w += b2.w;
    }
    sh[c4] = rbf(a.x); sh[c4 + 1] = rbf(a.y); sh[c4 + 2] = rbf(a.z); sh[c4 + 3] = rbf(a.w);
    sc[c4] = rbf(1.f + rbf(b.x)); sc[c4 + 1] = rbf(1.f + rbf(b.y));
    sc[c4 + 2] = rbf(1.f + rbf(b.z)); sc[c4 + 3] = rbf(1.f + rbf(b.w));
  }
  __syncthreads();
  const int wid = t >> 6, lane = t & 63;
#pragma unroll
  for (int rr = 0; rr < 2; ++rr) {
    const int row = blockIdx.x * 8 + wid * 2 + rr;
    const u16* xr = x + (size_t)row * D;
    uint4 v0 = *reinterpret_cast<const uint4*>(xr + lane * 8);
    uint4 v1 = *reinterpret_cast<const uint4*>(xr + 512 + lane * 8);
    float f[16];
    f[0] = lo16(v0.x); f[1] = hi16(v0.x); f[2] = lo16(v0.y); f[3] = hi16(v0.y);
    f[4] = lo16(v0.z); f[5] = hi16(v0.z); f[6] = lo16(v0.w); f[7] = hi16(v0.w);
    f[8] = lo16(v1.x); f[9] = hi16(v1.x); f[10] = lo16(v1.y); f[11] = hi16(v1.y);
    f[12] = lo16(v1.z); f[13] = hi16(v1.z); f[14] = lo16(v1.w); f[15] = hi16(v1.w);
    float s = 0.f;
#pragma unroll
    for (int j = 0; j < 16; ++j) s += f[j];
#pragma unroll
    for (int m = 1; m < 64; m <<= 1) s += __shfl_xor(s, m);
    const float mean = s * (1.f / D);
    float q = 0.f;
#pragma unroll
    for (int j = 0; j < 16; ++j) { float d = f[j] - mean; q += d * d; }
#pragma unroll
    for (int m = 1; m < 64; m <<= 1) q += __shfl_xor(q, m);
    const float rstd = rsqrtf(q * (1.f / D) + 1e-5f);
    unsigned o[8];
#pragma unroll
    for (int j = 0; j < 16; j += 2) {
      const int c0 = (j < 8) ? lane * 8 + j : 512 + lane * 8 + (j - 8);
      float a = rbf((f[j] - mean) * rstd) * sc[c0] + sh[c0];
      float b = rbf((f[j + 1] - mean) * rstd) * sc[c0 + 1] + sh[c0 + 1];
      o[j >> 1] = pack2(a, b);
    }
    u16* hr = h + (size_t)row * D;
    *reinterpret_cast<uint4*>(hr + lane * 8) = make_uint4(o[0], o[1], o[2], o[3]);
    *reinterpret_cast<uint4*>(hr + 512 + lane * 8) = make_uint4(o[4], o[5], o[6], o[7]);
  }
}

template <int BM, int NB, typename ColMap>
DI void gemm_core(const u16* __restrict__ X, const u16* __restrict__ W, int ldw, int m0, ColMap colmap, char* lds,
                  f32x4 (&acc)[NB][BM / 64]) {
  constexpr int MT = BM / 64;
  constexpr int XB = BM * 128;
  constexpr int WB = 8 * NB * 256;
  constexpr int STG = XB + WB;
  constexpr int XI = BM / 8 / 4;
  constexpr int WI = (8 * NB / 4) / 4;
  static_assert((8 * NB) % 16 == 0, "NB must be even");
  const int t = threadIdx.x, wid = t >> 6, lane = t & 63;
  const int l15 = lane & 15, g = lane >> 4;

  const u16* xsrc[XI];
  int xdst[XI];
#pragma unroll
  for (int i = 0; i < XI; ++i) {
    const int r0 = (wid * XI + i) * 8;
    const int row = r0 + (lane >> 3);
    const int c = (lane & 7) ^ ((row >> 1) & 7);
    xsrc[i] = X + (size_t)(m0 + row) * D + c * 8;
    xdst[i] = r0 * 128;
  }
  const u16* wsrc[WI];
  int wdst[WI];
#pragma unroll
  for (int i = 0; i < WI; ++i) {
    const int s0 = (wid * WI + i) * 4;
    const int s = s0 + (lane >> 4);
    const int kb = s / NB, nb = s % NB;
    const int pr = (lane & 15) >> 1, half = lane & 1;
    const int row = pr ^ (4 * (kb & 1));
    wsrc[i] = W + (size_t)(8 * kb + row) * ldw + colmap(nb) + 8 * half;
    wdst[i] = XB + s0 * 256;
  }
  auto stage = [&](int buf, int kt) {
    char* base = lds + buf * STG;
#pragma unroll
    for (int i = 0; i < XI; ++i)
      __builtin_amdgcn_global_load_lds((const unsigned*)(xsrc[i] + kt * 64), (LDSP(unsigned))(base + xdst[i]), 16, 0, 0);
#pragma unroll
    for (int i = 0; i < WI; ++i)
      __builtin_amdgcn_global_load_lds((const unsigned*)(wsrc[i] + (size_t)kt * 64 * ldw), (LDSP(unsigned))(base + wdst[i]), 16, 0, 0);
  };
  constexpr int NK = D / 64;
  stage(0, 0);
  stage(1, 1);
  for (int kt = 0; kt < NK; ++kt) {
    if (kt + 1 < NK) { asm volatile("s_waitcnt vmcnt(%0)" ::"i"(XI + WI) : "memory"); }
    else             { asm volatile("s_waitcnt vmcnt(0)" ::: "memory"); }
    __builtin_amdgcn_s_barrier();
    if (kt + 2 < NK) stage((kt + 2) % 3, kt + 2);
    const char* base = lds + (kt % 3) * STG;
#pragma unroll
    for (int kk = 0; kk < 2; ++kk) {
      bf16x8 bfr[MT];
#pragma unroll
      for (int mt = 0; mt < MT; ++mt) {
        const int row = wid * (BM / 4) + mt * 16 + l15;
        const int c = (kk * 4 + g) ^ ((row >> 1) & 7);
        bfr[mt] = *reinterpret_cast<const bf16x8*>(base + row * 128 + c * 16);
      }
      const int kb = kk * 4 + g;
      const int q = l15 >> 2, p = lane & 3;
#pragma unroll
      for (int nb = 0; nb < NB; ++nb) {
        const char* sp = base + XB + (kb * NB + nb) * 256 + p * 8;
        s16x4 a0 = __builtin_amdgcn_ds_read_tr16_b64_v4i16((LDSP(s16x4))(sp + ((q) ^ (4 * (kb & 1))) * 32));
        s16x4 a1 = __builtin_amdgcn_ds_read_tr16_b64_v4i16((LDSP(s16x4))(sp + ((4 + q) ^ (4 * (kb & 1))) * 32));
        bf16x8 afr = __builtin_shufflevector(a0, a1, 0, 1, 2, 3, 4, 5, 6, 7);
#pragma unroll
        for (int mt = 0; mt < MT; ++mt)
          acc[nb][mt] = __builtin_amdgcn_mfma_f32_16x16x32_bf16(afr, bfr[mt], acc[nb][mt], 0, 0, 0);
      }
    }
    asm volatile("s_waitcnt lgkmcnt(0)" ::: "memory");
  }
  __builtin_amdgcn_s_barrier();
}

DI void softmax_pm(const f32x4& p0, const f32x4& p1, float (&ep)[8], float (&en)[8]) {
  float v[8] = {rbf(p0[0]), rbf(p0[1]), rbf(p0[2]), rbf(p0[3]), rbf(p1[0]), rbf(p1[1]), rbf(p1[2]), rbf(p1[3])};
  float mx = v[0], mn = v[0];
#pragma unroll
  for (int j = 1; j < 8; ++j) { mx = fmaxf(mx, v[j]); mn = fminf(mn, v[j]); }
  mx = fmaxf(mx, __shfl_xor(mx, 16)); mx = fmaxf(mx, __shfl_xor(mx, 32));
  mn = fminf(mn, __shfl_xor(mn, 16)); mn = fminf(mn, __shfl_xor(mn, 32));
  float sp = 0.f, sn = 0.f;
#pragma unroll
  for (int j = 0; j < 8; ++j) {
    ep[j] = __expf(v[j] - mx); en[j] = __expf(mn - v[j]);
    sp += ep[j]; sn += en[j];
  }
  sp += __shfl_xor(sp, 16); sp += __shfl_xor(sp, 32);
  sn += __shfl_xor(sn, 16); sn += __shfl_xor(sn, 32);
  const float rp = 1.f / sp, rn = 1.f / sn;
#pragma unroll
  for (int j = 0; j < 8; ++j) { ep[j] *= rp; en[j] *= rn; }
}

__global__ __launch_bounds__(256) void k_qkv(const u16* __restrict__ h1, const u16* __restrict__ wqkv,
                                             const u16* __restrict__ wphi, u16* __restrict__ phq,
                                             float* __restrict__ kvp, float* __restrict__ zp) {
  extern __shared__ __attribute__((aligned(16))) char lds[];
  const int b = blockIdx.x, xcd = b & 7, idx = b >> 3;
  const int mtile = 2 * (xcd & 3) + (idx & 1), head = 16 * (xcd >> 2) + (idx >> 1);
  const int m0 = mtile * 256;
  const int t = threadIdx.x, wid = t >> 6, lane = t & 63, l15 = lane & 15, g = lane >> 4;
  f32x4 acc[6][4];
#pragma unroll
  for (int i = 0; i < 6; ++i)
#pragma unroll
    for (int j = 0; j < 4; ++j) acc[i][j] = (f32x4){0.f, 0.f, 0.f, 0.f};
  gemm_core<256, 6>(h1, wqkv, 3 * D, m0, [&](int nb) { return (nb >> 1) * D + head * 32 + (nb & 1) * 16; }, lds, acc);

  bf16x8 aphi[2];
#pragma unroll
  for (int ft = 0; ft < 2; ++ft)
#pragma unroll
    for (int j = 0; j < 8; ++j) {
      const int d = (j < 4) ? 4 * g + j : 16 + 4 * g + (j - 4);
      aphi[ft][j] = (short)wphi[(head * 32 + d) * 32 + 16 * ft + l15];
    }
  char* pk_img = lds;
  char* v_img = lds + 256 * 128;
#pragma unroll
  for (int mt = 0; mt < 4; ++mt) {
    const int m = wid * 64 + mt * 16 + l15;
    {
      bf16x8 bq;
#pragma unroll
      for (int j = 0; j < 4; ++j) { bq[j] = (short)f2bf(acc[0][mt][j]); bq[4 + j] = (short)f2bf(acc[1][mt][j]); }
      f32x4 z4 = {0.f, 0.f, 0.f, 0.f};
      f32x4 p0 = __builtin_amdgcn_mfma_f32_16x16x32_bf16(aphi[0], bq, z4, 0, 0, 0);
      f32x4 p1 = __builtin_amdgcn_mfma_f32_16x16x32_bf16(aphi[1], bq, z4, 0, 0, 0);
      float ep[8], en[8];
      softmax_pm(p0, p1, ep, en);
      u16* dst = phq + ((size_t)(m0 + m) * H + head) * 64 + 4 * g;
      *reinterpret_cast<uint2*>(dst)      = make_uint2(pack2(ep[0], ep[1]), pack2(ep[2], ep[3]));
      *reinterpret_cast<uint2*>(dst + 16) = make_uint2(pack2(ep[4], ep[5]), pack2(ep[6], ep[7]));
      *reinterpret_cast<uint2*>(dst + 32) = make_uint2(pack2(en[0], en[1]), pack2(en[2], en[3]));
      *reinterpret_cast<uint2*>(dst + 48) = make_uint2(pack2(en[4], en[5]), pack2(en[6], en[7]));
    }
    {
      bf16x8 bk;
#pragma unroll
      for (int j = 0; j < 4; ++j) { bk[j] = (short)f2bf(acc[2][mt][j]); bk[4 + j] = (short)f2bf(acc[3][mt][j]); }
      f32x4 z4 = {0.f, 0.f, 0.f, 0.f};
      f32x4 p0 = __builtin_amdgcn_mfma_f32_16x16x32_bf16(aphi[0], bk, z4, 0, 0, 0);
      f32x4 p1 = __builtin_amdgcn_mfma_f32_16x16x32_bf16(aphi[1], bk, z4, 0, 0, 0);
      float ep[8], en[8];
      softmax_pm(p0, p1, ep, en);
      char* dst = pk_img + m * 128 + 8 * g;
      *reinterpret_cast<uint2*>(dst)      = make_uint2(pack2(ep[0], ep[1]), pack2(ep[2], ep[3]));
      *reinterpret_cast<uint2*>(dst + 32) = make_uint2(pack2(ep[4], ep[5]), pack2(ep[6], ep[7]));
      *reinterpret_cast<uint2*>(dst + 64) = make_uint2(pack2(en[0], en[1]), pack2(en[2], en[3]));
      *reinterpret_cast<uint2*>(dst + 96) = make_uint2(pack2(en[4], en[5]), pack2(en[6], en[7]));
    }
    {
      char* dst = v_img + m * 64 + 8 * g;
      *reinterpret_cast<uint2*>(dst)      = make_uint2(pack2(acc[4][mt][0], acc[4][mt][1]), pack2(acc[4][mt][2], acc[4][mt][3]));
      *reinterpret_cast<uint2*>(dst + 32) = make_uint2(pack2(acc[5][mt][0], acc[5][mt][1]), pack2(acc[5][mt][2], acc[5][mt][3]));
    }
  }
  __syncthreads();
  {
    f32x4 c0 = {0.f, 0.f, 0.f, 0.f}, c1 = c0, cz = c0;
    bf16x8 ones;
#pragma unroll
    for (int j = 0; j < 8; ++j) ones[j] = (short)0x3f80;
    const int q = l15 >> 2, p = lane & 3;
#pragma unroll
    for (int ks = 0; ks < 8; ++ks) {
      const int r0 = ks * 32 + 8 * g + q;
      s16x4 a0 = __builtin_amdgcn_ds_read_tr16_b64_v4i16((LDSP(s16x4))(pk_img + (r0) * 128 + (16 * wid + 4 * p) * 2));
      s16x4 a1 = __builtin_amdgcn_ds_read_tr16_b64_v4i16((LDSP(s16x4))(pk_img + (r0 + 4) * 128 + (16 * wid + 4 * p) * 2));
      bf16x8 afr = __builtin_shufflevector(a0, a1, 0, 1, 2, 3, 4, 5, 6, 7);
      s16x4 b0 = __builtin_amdgcn_ds_read_tr16_b64_v4i16((LDSP(s16x4))(v_img + (r0) * 64 + (4 * p) * 2));
      s16x4 b1 = __builtin_amdgcn_ds_read_tr16_b64_v4i16((LDSP(s16x4))(v_img + (r0 + 4) * 64 + (4 * p) * 2));
      bf16x8 bf0 = __builtin_shufflevector(b0, b1, 0, 1, 2, 3, 4, 5, 6, 7);
      s16x4 b2 = __builtin_amdgcn_ds_read_tr16_b64_v4i16((LDSP(s16x4))(v_img + (r0) * 64 + (16 + 4 * p) * 2));
      s16x4 b3 = __builtin_amdgcn_ds_read_tr16_b64_v4i16((LDSP(s16x4))(v_img + (r0 + 4) * 64 + (16 + 4 * p) * 2));
      bf16x8 bf1 = __builtin_shufflevector(b2, b3, 0, 1, 2, 3, 4, 5, 6, 7);
      c0 = __builtin_amdgcn_mfma_f32_16x16x32_bf16(afr, bf0, c0, 0, 0, 0);
      c1 = __builtin_amdgcn_mfma_f32_16x16x32_bf16(afr, bf1, c1, 0, 0, 0);
      cz = __builtin_amdgcn_mfma_f32_16x16x32_bf16(afr, ones, cz, 0, 0, 0);
    }
    float* kd = kvp + ((size_t)(mtile * H + head) * 64) * 32;
#pragma unroll
    for (int r = 0; r < 4; ++r) {
      const int f = 16 * wid + 4 * g + r;
      kd[f * 32 + l15] = c0[r];
      kd[f * 32 + 16 + l15] = c1[r];
      if (l15 == 0) zp[(mtile * H + head) * 64 + f] = cz[r];
    }
  }
}

__global__ __launch_bounds__(256) void k_attn_o(const u16* __restrict__ phq, const float* __restrict__ kvp,
                                                const float* __restrict__ zp, u16* __restrict__ o) {
  __shared__ __attribute__((aligned(16))) u16 kvT[32][64 + 8];
  __shared__ float zs[64];
  const int head = blockIdx.x & 31, chunk = blockIdx.x >> 5;
  const int t = threadIdx.x, wid = t >> 6, lane = t & 63, l15 = lane & 15, g = lane >> 4;
  {
    const int f = t >> 2, d0 = (t & 3) * 8;
    float s[8];
#pragma unroll
    for (int j = 0; j < 8; ++j) s[j] = 0.f;
#pragma unroll
    for (int p = 0; p < 8; ++p) {
      const float* src = kvp + ((size_t)(p * H + head) * 64 + f) * 32 + d0;
      float4 a = *reinterpret_cast<const float4*>(src);
      float4 b2 = *reinterpret_cast<const float4*>(src + 4);
      s[0] += a.x; s[1] += a.y; s[2] += a.z; s[3] += a.w;
      s[4] += b2.x; s[5] += b2.y; s[6] += b2.z; s[7] += b2.w;
    }
#pragma unroll
    for (int j = 0; j < 8; ++j) kvT[d0 + j][f] = f2bf(s[j]);
    if (t < 64) {
      float z = 0.f;
#pragma unroll
      for (int p = 0; p < 8; ++p) z += zp[(p * H + head) * 64 + t];
      zs[t] = rbf(z);
    }
  }
  __syncthreads();
  bf16x8 af[2][2];
#pragma unroll
  for (int dt = 0; dt < 2; ++dt)
#pragma unroll
    for (int kk = 0; kk < 2; ++kk)
      af[dt][kk] = *reinterpret_cast<const bf16x8*>(&kvT[16 * dt + l15][32 * kk + 8 * g]);
  float zr[2][8];
#pragma unroll
  for (int kk = 0; kk < 2; ++kk)
#pragma unroll
    for (int j = 0; j < 8; ++j) zr[kk][j] = zs[32 * kk + 8 * g + j];
#pragma unroll
  for (int mt = 0; mt < 4; ++mt) {
    const int m = chunk * 256 + wid * 64 + mt * 16 + l15;
    const u16* src = phq + ((size_t)m * H + head) * 64 + 8 * g;
    bf16x8 b0 = *reinterpret_cast<const bf16x8*>(src);
    bf16x8 b1 = *reinterpret_cast<const bf16x8*>(src + 32);
    float den = 0.f;
#pragma unroll
    for (int j = 0; j < 8; ++j) den += bf2f((u16)b0[j]) * zr[0][j] + bf2f((u16)b1[j]) * zr[1][j];
    den += __shfl_xor(den, 16); den += __shfl_xor(den, 32);
    den = rbf(rbf(den) + 1e-6f);
    const float rden = 1.f / den;
#pragma unroll
    for (int dt = 0; dt < 2; ++dt) {
      f32x4 c = {0.f, 0.f, 0.f, 0.f};
      c = __builtin_amdgcn_mfma_f32_16x16x32_bf16(af[dt][0], b0, c, 0, 0, 0);
      c = __builtin_amdgcn_mfma_f32_16x16x32_bf16(af[dt][1], b1, c, 0, 0, 0);
      *reinterpret_cast<uint2*>(o + (size_t)m * D + head * 32 + 16 * dt + 4 * g) =
          make_uint2(pack2(rbf(c[0]) * rden, rbf(c[1]) * rden), pack2(rbf(c[2]) * rden, rbf(c[3]) * rden));
    }
  }
}

__global__ __launch_bounds__(256) void k_oproj(const u16* __restrict__ o, const u16* __restrict__ wo,
                                               const u16* __restrict__ x, const float* __restrict__ modpart,
                                               u16* __restrict__ x1) {
  extern __shared__ __attribute__((aligned(16))) char lds[];
  const int b = blockIdx.x, xcd = b & 7, idx = b >> 3;
  const int mtile = 2 * xcd + (idx & 1), ntile = idx >> 1;
  const int m0 = mtile * 128, n0 = ntile * 64;
  const int t = threadIdx.x, wid = t >> 6, lane = t & 63, l15 = lane & 15, g = lane >> 4;
  f32x4 acc[4][2];
#pragma unroll
  for (int i = 0; i < 4; ++i)
#pragma unroll
    for (int j = 0; j < 2; ++j) acc[i][j] = (f32x4){0.f, 0.f, 0.f, 0.f};
  gemm_core<128, 4>(o, wo, D, m0, [&](int nb) { return n0 + nb * 16; }, lds, acc);
#pragma unroll
  for (int nb = 0; nb < 4; ++nb) {
    const int n = n0 + nb * 16 + 4 * g;
    float ga[4];
#pragma unroll
    for (int v = 0; v < 2; ++v) {}
    {
      float4 a = *reinterpret_cast<const float4*>(modpart + 2 * D + n);
#pragma unroll
      for (int p = 1; p < 4; ++p) {
        float4 a2 = *reinterpret_cast<const float4*>(modpart + p * 6144 + 2 * D + n);
        a.x += a2.x; a.y += a2.y; a.z += a2.z; a.w += a2.w;
      }
      ga[0] = rbf(a.x); ga[1] = rbf(a.y); ga[2] = rbf(a.z); ga[3] = rbf(a.w);
    }
#pragma unroll
    for (int mt = 0; mt < 2; ++mt) {
      const int m = m0 + wid * 32 + mt * 16 + l15;
      uint2 xv = *reinterpret_cast<const uint2*>(x + (size_t)m * D + n);
      float r0 = lo16(xv.x) + rbf(ga[0] * rbf(acc[nb][mt][0]));
      float r1 = hi16(xv.x) + rbf(ga[1] * rbf(acc[nb][mt][1]));
      float r2 = lo16(xv.y) + rbf(ga[2] * rbf(acc[nb][mt][2]));
      float r3 = hi16(xv.y) + rbf(ga[3] * rbf(acc[nb][mt][3]));
      *reinterpret_cast<uint2*>(x1 + (size_t)m * D + n) = make_uint2(pack2(r0, r1), pack2(r2, r3));
    }
  }
}

__global__ __launch_bounds__(256) void k_ln2(const u16* __restrict__ x1, const float* __restrict__ modpart,
                                             const u16* __restrict__ wr, u16* __restrict__ h2,
                                             u16* __restrict__ out, float* __restrict__ r) {
  __shared__ float sc[D], sh[D], gbv[D];
  const int t = threadIdx.x;
  {
    const int c4 = t * 4;
    float4 a = *reinterpret_cast<const float4*>(modpart + 3 * D + c4);
    float4 b = *reinterpret_cast<const float4*>(modpart + 4 * D + c4);
    float4 c = *reinterpret_cast<const float4*>(modpart + 5 * D + c4);
#pragma unroll
    for (int p = 1; p < 4; ++p) {
      float4 a2 = *reinterpret_cast<const float4*>(modpart + p * 6144 + 3 * D + c4);
      float4 b2 = *reinterpret_cast<const float4*>(modpart + p * 6144 + 4 * D + c4);
      float4 c2 = *reinterpret_cast<const float4*>(modpart + p * 6144 + 5 * D + c4);
      a.x += a2.x; a.y += a2.y; a.z += a2.z; a.w += a2.w;
      b.x += b2.x; b.y += b2.y; b.z += b2.z; b.w += b2.w;
      c.x += c2.x; c.y += c2.y; c.z += c2.z; c.w += c2.w;
    }
    sh[c4] = rbf(a.x); sh[c4 + 1] = rbf(a.y); sh[c4 + 2] = rbf(a.z); sh[c4 + 3] = rbf(a.w);
    sc[c4] = rbf(1.f + rbf(b.x)); sc[c4 + 1] = rbf(1.f + rbf(b.y));
    sc[c4 + 2] = rbf(1.f + rbf(b.z)); sc[c4 + 3] = rbf(1.f + rbf(b.w));
    gbv[c4] = rbf(c.x); gbv[c4 + 1] = rbf(c.y); gbv[c4 + 2] = rbf(c.z); gbv[c4 + 3] = rbf(c.w);
  }
  __syncthreads();
  const int wid = t >> 6, lane = t & 63;
  uint4 w0 = *reinterpret_cast<const uint4*>(wr + lane * 8);
  uint4 w1 = *reinterpret_cast<const uint4*>(wr + 512 + lane * 8);
  float wf[16];
  wf[0] = lo16(w0.x); wf[1] = hi16(w0.x); wf[2] = lo16(w0.y); wf[3] = hi16(w0.y);
  wf[4] = lo16(w0.z); wf[5] = hi16(w0.z); wf[6] = lo16(w0.w); wf[7] = hi16(w0.w);
  wf[8] = lo16(w1.x); wf[9] = hi16(w1.x); wf[10] = lo16(w1.y); wf[11] = hi16(w1.y);
  wf[12] = lo16(w1.z); wf[13] = hi16(w1.z); wf[14] = lo16(w1.w); wf[15] = hi16(w1.w);
#pragma unroll
  for (int rr = 0; rr < 2; ++rr) {
    const int row = blockIdx.x * 8 + wid * 2 + rr;
    const u16* xr = x1 + (size_t)row * D;
    uint4 v0 = *reinterpret_cast<const uint4*>(xr + lane * 8);
    uint4 v1 = *reinterpret_cast<const uint4*>(xr + 512 + lane * 8);
    float f[16];
    f[0] = lo16(v0.x); f[1] = hi16(v0.x); f[2] = lo16(v0.y); f[3] = hi16(v0.y);
    f[4] = lo16(v0.z); f[5] = hi16(v0.z); f[6] = lo16(v0.w); f[7] = hi16(v0.w);
    f[8] = lo16(v1.x); f[9] = hi16(v1.x); f[10] = lo16(v1.y); f[11] = hi16(v1.y);
    f[12] = lo16(v1.z); f[13] = hi16(v1.z); f[14] = lo16(v1.w); f[15] = hi16(v1.w);
    float s = 0.f;
#pragma unroll
    for (int j = 0; j < 16; ++j) s += f[j];
#pragma unroll
    for (int m = 1; m < 64; m <<= 1) s += __shfl_xor(s, m);
    const float mean = s * (1.f / D);
    float q = 0.f;
#pragma unroll
    for (int j = 0; j < 16; ++j) { float d = f[j] - mean; q += d * d; }
#pragma unroll
    for (int m = 1; m < 64; m <<= 1) q += __shfl_xor(q, m);
    const float rstd = rsqrtf(q * (1.f / D) + 1e-5f);
    unsigned ho[8], oo[8];
    float dot = 0.f;
#pragma unroll
    for (int j = 0; j < 16; j += 2) {
      const int c0 = (j < 8) ? lane * 8 + j : 512 + lane * 8 + (j - 8);
      float a = rbf(rbf((f[j] - mean) * rstd) * sc[c0] + sh[c0]);
      float b = rbf(rbf((f[j + 1] - mean) * rstd) * sc[c0 + 1] + sh[c0 + 1]);
      dot += a * wf[j] + b * wf[j + 1];
      ho[j >> 1] = pack2(a, b);
      oo[j >> 1] = pack2(f[j] + rbf(gbv[c0] * a), f[j + 1] + rbf(gbv[c0 + 1] * b));
    }
#pragma unroll
    for (int m = 1; m < 64; m <<= 1) dot += __shfl_xor(dot, m);
    u16* hr = h2 + (size_t)row * D;
    *reinterpret_cast<uint4*>(hr + lane * 8) = make_uint4(ho[0], ho[1], ho[2], ho[3]);
    *reinterpret_cast<uint4*>(hr + 512 + lane * 8) = make_uint4(ho[4], ho[5], ho[6], ho[7]);
    u16* orow = out + (size_t)row * D;
    *reinterpret_cast<uint4*>(orow + lane * 8) = make_uint4(oo[0], oo[1], oo[2], oo[3]);
    *reinterpret_cast<uint4*>(orow + 512 + lane * 8) = make_uint4(oo[4], oo[5], oo[6], oo[7]);
    if (lane == 0) r[row] = rbf(dot);
  }
}

DI void top2(const float* __restrict__ r, int& i0, int& i1, float& r0, float& r1, float* sm_v, int* sm_i) {
  const int t = threadIdx.x;
  float b0 = -INFINITY, b1 = -INFINITY; int j0 = 0x7fffffff, j1 = 0x7fffffff;
#pragma unroll
  for (int i = 0; i < 8; ++i) {
    const int idx = t + i * 256;
    const float v = r[idx];
    if (v > b0 || (v == b0 && idx < j0)) { b1 = b0; j1 = j0; b0 = v; j0 = idx; }
    else if (v > b1 || (v == b1 && idx < j1)) { b1 = v; j1 = idx; }
  }
#pragma unroll
  for (int m = 1; m < 64; m <<= 1) {
    float o0 = __shfl_xor(b0, m), o1 = __shfl_xor(b1, m);
    int p0 = __shfl_xor(j0, m), p1 = __shfl_xor(j1, m);
    if (o0 > b0 || (o0 == b0 && p0 < j0)) {
      if (b0 > o1 || (b0 == o1 && j0 < p1)) { b1 = b0; j1 = j0; } else { b1 = o1; j1 = p1; }
      b0 = o0; j0 = p0;
    } else {
      if (o0 > b1 || (o0 == b1 && p0 < j1)) { b1 = o0; j1 = p0; }
    }
  }
  if ((t & 63) == 0) { sm_v[(t >> 6) * 2] = b0; sm_v[(t >> 6) * 2 + 1] = b1; sm_i[(t >> 6) * 2] = j0; sm_i[(t >> 6) * 2 + 1] = j1; }
  __syncthreads();
  b0 = sm_v[0]; b1 = sm_v[1]; j0 = sm_i[0]; j1 = sm_i[1];
#pragma unroll
  for (int w = 1; w < 4; ++w) {
    float o0 = sm_v[w * 2], o1 = sm_v[w * 2 + 1];
    int p0 = sm_i[w * 2], p1 = sm_i[w * 2 + 1];
    if (o0 > b0 || (o0 == b0 && p0 < j0)) {
      if (b0 > o1 || (b0 == o1 && j0 < p1)) { b1 = b0; j1 = j0; } else { b1 = o1; j1 = p1; }
      b0 = o0; j0 = p0;
    } else {
      if (o0 > b1 || (o0 == b1 && p0 < j1)) { b1 = o0; j1 = p0; }
    }
  }
  i0 = j0; i1 = j1; r0 = b0; r1 = b1;
}

__global__ __launch_bounds__(256) void k_gateup(const float* __restrict__ r, const u16* __restrict__ h2,
                                                const u16* __restrict__ wg, const u16* __restrict__ wu,
                                                float* __restrict__ gup) {
  __shared__ float red[32][65];
  __shared__ float sm_v[8]; __shared__ int sm_i[8];
  const int cc = blockIdx.x % 56, ks = blockIdx.x / 56;
  const int t = threadIdx.x, cq = t & 7, rr = t >> 3;
  const int col0 = cc * 64 + cq * 8;
  uint4 wa[8], wb[8];
#pragma unroll
  for (int i = 0; i < 8; ++i) {
    const int k = ks * 256 + i * 32 + rr;
    wa[i] = *reinterpret_cast<const uint4*>(wg + (size_t)k * F + col0);
    wb[i] = *reinterpret_cast<const uint4*>(wu + (size_t)k * F + col0);
  }
  int i0, i1; float r0, r1;
  top2(r, i0, i1, r0, r1, sm_v, sm_i);
  float acc[4][8];
#pragma unroll
  for (int a = 0; a < 4; ++a)
#pragma unroll
    for (int j = 0; j < 8; ++j) acc[a][j] = 0.f;
#pragma unroll
  for (int i = 0; i < 8; ++i) {
    const int k = ks * 256 + i * 32 + rr;
    const float h0 = bf2f(h2[(size_t)i0 * D + k]), h1v = bf2f(h2[(size_t)i1 * D + k]);
    const unsigned wv[4] = {wa[i].x, wa[i].y, wa[i].z, wa[i].w};
    const unsigned uv[4] = {wb[i].x, wb[i].y, wb[i].z, wb[i].w};
#pragma unroll
    for (int j = 0; j < 4; ++j) {
      acc[0][2 * j] += h0 * lo16(wv[j]);  acc[0][2 * j + 1] += h0 * hi16(wv[j]);
      acc[1][2 * j] += h1v * lo16(wv[j]); acc[1][2 * j + 1] += h1v * hi16(wv[j]);
      acc[2][2 * j] += h0 * lo16(uv[j]);  acc[2][2 * j + 1] += h0 * hi16(uv[j]);
      acc[3][2 * j] += h1v * lo16(uv[j]); acc[3][2 * j + 1] += h1v * hi16(uv[j]);
    }
  }
#pragma unroll
  for (int a = 0; a < 4; ++a) {
    __syncthreads();
#pragma unroll
    for (int j = 0; j < 8; ++j) red[rr][cq * 8 + j] = acc[a][j];
    __syncthreads();
    if (t < 64) {
      float s = 0.f;
#pragma unroll
      for (int i = 0; i < 32; ++i) s += red[i][t];
      gup[(size_t)(ks * 4 + a) * F + cc * 64 + t] = s;
    }
  }
}

__global__ __launch_bounds__(256) void k_down(const float* __restrict__ gup, const u16* __restrict__ wd,
                                              float* __restrict__ dpart) {
  __shared__ float red[32][65];
  __shared__ float hs[2][256];
  const int cc = blockIdx.x & 15, ks = blockIdx.x >> 4;
  const int t = threadIdx.x, cq = t & 7, rr = t >> 3;
  const int col0 = cc * 64 + cq * 8;
  uint4 wa[8];
#pragma unroll
  for (int i = 0; i < 8; ++i) {
    const int k = ks * 256 + i * 32 + rr;
    wa[i] = *reinterpret_cast<const uint4*>(wd + (size_t)k * D + col0);
  }
  {
    const int k = ks * 256 + t;
#pragma unroll
    for (int tok = 0; tok < 2; ++tok) {
      float gsum = 0.f, usum = 0.f;
#pragma unroll
      for (int p = 0; p < 4; ++p) {
        gsum += gup[(size_t)(p * 4 + tok) * F + k];
        usum += gup[(size_t)(p * 4 + 2 + tok) * F + k];
      }
      gsum = rbf(gsum); usum = rbf(usum);
      const float sg = rbf(gsum / (1.f + __expf(-gsum)));
      hs[tok][t] = rbf(sg * usum);
    }
  }
  __syncthreads();
  float acc[2][8];
#pragma unroll
  for (int a = 0; a < 2; ++a)
#pragma unroll
    for (int j = 0; j < 8; ++j) acc[a][j] = 0.f;
#pragma unroll
  for (int i = 0; i < 8; ++i) {
    const int kl = i * 32 + rr;
    const float h0 = hs[0][kl], h1v = hs[1][kl];
    const unsigned wv[4] = {wa[i].x, wa[i].y, wa[i].z, wa[i].w};
#pragma unroll
    for (int j = 0; j < 4; ++j) {
      acc[0][2 * j] += h0 * lo16(wv[j]);  acc[0][2 * j + 1] += h0 * hi16(wv[j]);
      acc[1][2 * j] += h1v * lo16(wv[j]); acc[1][2 * j + 1] += h1v * hi16(wv[j]);
    }
  }
#pragma unroll
  for (int a = 0; a < 2; ++a) {
    __syncthreads();
#pragma unroll
    for (int j = 0; j < 8; ++j) red[rr][cq * 8 + j] = acc[a][j];
    __syncthreads();
    if (t < 64) {
      float s = 0.f;
#pragma unroll
      for (int i = 0; i < 32; ++i) s += red[i][t];
      dpart[(size_t)(ks * 2 + a) * D + cc * 64 + t] = s;
    }
  }
}

__global__ __launch_bounds__(256) void k_final(const float* __restrict__ r, const float* __restrict__ dpart,
                                               const u16* __restrict__ x1, const u16* __restrict__ h2,
                                               const float* __restrict__ modpart, u16* __restrict__ out) {
  __shared__ float sm_v[8]; __shared__ int sm_i[8];
  int i0, i1; float r0, r1;
  top2(r, i0, i1, r0, r1, sm_v, sm_i);
  const int tok = blockIdx.x >> 2, n = (blockIdx.x & 3) * 256 + threadIdx.x;
  const int row = tok ? i1 : i0;
  const float rv = tok ? r1 : r0;
  float y = 0.f;
#pragma unroll
  for (int p = 0; p < 14; ++p) y += dpart[(size_t)(p * 2 + tok) * D + n];
  y = rbf(y);
  const float sig = rbf(1.f / (1.f + __expf(-rv)));
  y = rbf(y * sig);
  float gb = 0.f;
#pragma unroll
  for (int p = 0; p < 4; ++p) gb += modpart[p * 6144 + 5 * D + n];
  gb = rbf(gb);
  const float hv = bf2f(h2[(size_t)row * D + n]);
  const float xv = bf2f(x1[(size_t)row * D + n]);
  out[(size_t)row * D + n] = f2bf(xv + rbf(gb * rbf(hv + y)));
}

extern "C" void kernel_launch(void* const* d_in, const int* in_sizes, int n_in, void* d_out, int out_size,
                              void* d_ws, size_t ws_size, hipStream_t stream) {
  const u16* x    = (const u16*)d_in[0];
  const u16* wqkv = (const u16*)d_in[1];
  const u16* wphi = (const u16*)d_in[2];
  const u16* wo   = (const u16*)d_in[3];
  const u16* w_r  = (const u16*)d_in[5];
  const u16* wg   = (const u16*)d_in[6];
  const u16* wu   = (const u16*)d_in[7];
  const u16* wd   = (const u16*)d_in[8];
  const u16* c    = (const u16*)d_in[9];
  const u16* wmod = (const u16*)d_in[10];
  u16* out = (u16*)d_out;

  char* ws = (char*)d_ws;
  size_t off = 0;
  auto alloc = [&](size_t bytes) { char* p = ws + off; off += (bytes + 255) & ~(size_t)255; return p; };
  float* modpart = (float*)alloc(4 * 6144 * 4);
  u16* h1  = (u16*)alloc((size_t)S * D * 2);
  u16* phq = (u16*)alloc((size_t)S * H * 64 * 2);
  float* kvp = (float*)alloc((size_t)8 * H * 64 * 32 * 4);
  float* zp  = (float*)alloc((size_t)8 * H * 64 * 4);
  u16* o   = (u16*)alloc((size_t)S * D * 2);
  u16* x1  = (u16*)alloc((size_t)S * D * 2);
  u16* h2  = (u16*)alloc((size_t)S * D * 2);
  float* r = (float*)alloc(S * 4);
  float* gup = (float*)alloc((size_t)16 * F * 4);
  float* dpart = (float*)alloc((size_t)28 * D * 4);

  k_mod<<<384, 256, 0, stream>>>(c, wmod, modpart);
  k_ln1<<<256, 256, 0, stream>>>(x, modpart, h1);
  constexpr int LDS_QKV = 3 * (256 * 128 + 8 * 6 * 256);
  constexpr int LDS_OP  = 3 * (128 * 128 + 8 * 4 * 256);
  hipFuncSetAttribute((const void*)k_qkv, hipFuncAttributeMaxDynamicSharedMemorySize, LDS_QKV);
  hipFuncSetAttribute((const void*)k_oproj, hipFuncAttributeMaxDynamicSharedMemorySize, LDS_OP);
  k_qkv<<<256, 256, LDS_QKV, stream>>>(h1, wqkv, wphi, phq, kvp, zp);
  k_attn_o<<<256, 256, 0, stream>>>(phq, kvp, zp, o);
  k_oproj<<<256, 256, LDS_OP, stream>>>(o, wo, x, modpart, x1);
  k_ln2<<<256, 256, 0, stream>>>(x1, modpart, w_r, h2, out, r);
  k_gateup<<<224, 256, 0, stream>>>(r, h2, wg, wu, gup);
  k_down<<<224, 256, 0, stream>>>(gup, wd, dpart);
  k_final<<<8, 256, 0, stream>>>(r, dpart, x1, h2, modpart, out);
}
```

```cpp
#include <hip/hip_runtime.h>
#include <hip/hip_bf16.h>
#include <stdint.h>
#define OPST 3

constexpr int S  = 2048;
constexpr int D  = 1024;
constexpr int H  = 32;
constexpr int F  = 3584;

typedef short  s16x4  __attribute__((ext_vector_type(4)));
typedef short  bf16x8 __attribute__((ext_vector_type(8)));
typedef float  f32x4  __attribute__((ext_vector_type(4)));
typedef unsigned short u16;
#define LDSP(T) __attribute__((address_space(3))) T*
#define DI __device__ __forceinline__
#define STAMP(k, i) do {} while (0)

DI float bf2f(u16 x) { return __uint_as_float(((unsigned)x) << 16); }
typedef __bf16 bf16v2 __attribute__((ext_vector_type(2)));
typedef float f32v2 __attribute__((ext_vector_type(2)));
DI u16 f2bf(float f) { __bf16 b = (__bf16)f; return __builtin_bit_cast(u16, b); }
DI float rbf(float f) { return (float)(__bf16)f; }
DI unsigned pack2(float a, float b) {
  f32v2 v = {a, b};
  return __builtin_bit_cast(unsigned, __builtin_convertvector(v, bf16v2));
}
DI float lo16(unsigned u) { return __uint_as_float(u << 16); }
DI float hi16(unsigned u) { return __uint_as_float(u & 0xffff0000u); }

__global__ __launch_bounds__(256) void k_mod(const u16* __restrict__ c, const u16* __restrict__ wmod,
                                             float* __restrict__ part, unsigned* __restrict__ ctr) {
  if (blockIdx.x == 0 && threadIdx.x < 64) ctr[threadIdx.x] = 0u;
  __shared__ float red[32][65];
  const int cc = blockIdx.x % 96, ks = blockIdx.x / 96;
  const int t = threadIdx.x, cq = t & 7, rr = t >> 3;
  const int col0 = cc * 64 + cq * 8;
  uint4 w[8];
#pragma unroll
  for (int i = 0; i < 8; ++i) {
    const int k = ks * 256 + i * 32 + rr;
    w[i] = *reinterpret_cast<const uint4*>(wmod + (size_t)k * 6144 + col0);
  }
  float acc[8];
#pragma unroll
  for (int j = 0; j < 8; ++j) acc[j] = 0.f;
#pragma unroll
  for (int i = 0; i < 8; ++i) {
    const int k = ks * 256 + i * 32 + rr;
    const float cv = bf2f(c[k]);
    const float s = rbf(cv / (1.f + __expf(-cv)));
    acc[0] += s * lo16(w[i].x); acc[1] += s * hi16(w[i].x);
    acc[2] += s * lo16(w[i].y); acc[3] += s * hi16(w[i].y);
    acc[4] += s * lo16(w[i].z); acc[5] += s * hi16(w[i].z);
    acc[6] += s * lo16(w[i].w); acc[7] += s * hi16(w[i].w);
  }
#pragma unroll
  for (int j = 0; j < 8; ++j) red[rr][cq * 8 + j] = acc[j];
  __syncthreads();
  if (t < 64) {
    float s = 0.f;
#pragma unroll
    for (int i = 0; i < 32; ++i) s += red[i][t];
    part[ks * 6144 + cc * 64 + t] = s;
  }
}

__global__ __launch_bounds__(256) void k_ln1(const u16* __restrict__ x, const float* __restrict__ modpart,
                                             u16* __restrict__ h) {
  __shared__ float sc[D], sh[D];
  const int t = threadIdx.x;
  {
    const int c4 = t * 4;
    float4 a = *reinterpret_cast<const float4*>(modpart + c4);
    float4 b = *reinterpret_cast<const float4*>(modpart + D + c4);
#pragma unroll
    for (int p = 1; p < 4; ++p) {
      float4 a2 = *reinterpret_cast<const float4*>(modpart + p * 6144 + c4);
      float4 b2 = *reinterpret_cast<const float4*>(modpart + p * 6144 + D + c4);
      a.x += a2.x; a.y += a2.y; a.z += a2.z; a.w += a2.w;
      b.x += b2.x; b.y += b2.y; b.z += b2.z; b.w += b2.w;
    }
    sh[c4] = rbf(a.x); sh[c4 + 1] = rbf(a.y); sh[c4 + 2] = rbf(a.z); sh[c4 + 3] = rbf(a.w);
    sc[c4] = rbf(1.f + rbf(b.x)); sc[c4 + 1] = rbf(1.f + rbf(b.y));
    sc[c4 + 2] = rbf(1.f + rbf(b.z)); sc[c4 + 3] = rbf(1.f + rbf(b.w));
  }
  __syncthreads();
  const int wid = t >> 6, lane = t & 63;
#pragma unroll
  for (int rr = 0; rr < 2; ++rr) {
    const int row = blockIdx.x * 8 + wid * 2 + rr;
    const u16* xr = x + (size_t)row * D;
    uint4 v0 = *reinterpret_cast<const uint4*>(xr + lane * 8);
    uint4 v1 = *reinterpret_cast<const uint4*>(xr + 512 + lane * 8);
    float f[16];
    f[0] = lo16(v0.x); f[1] = hi16(v0.x); f[2] = lo16(v0.y); f[3] = hi16(v0.y);
    f[4] = lo16(v0.z); f[5] = hi16(v0.z); f[6] = lo16(v0.w); f[7] = hi16(v0.w);
    f[8] = lo16(v1.x); f[9] = hi16(v1.x); f[10] = lo16(v1.y); f[11] = hi16(v1.y);
    f[12] = lo16(v1.z); f[13] = hi16(v1.z); f[14] = lo16(v1.w); f[15] = hi16(v1.w);
    float s = 0.f;
#pragma unroll
    for (int j = 0; j < 16; ++j) s += f[j];
#pragma unroll
    for (int m = 1; m < 64; m <<= 1) s += __shfl_xor(s, m);
    const float mean = s * (1.f / D);
    float q = 0.f;
#pragma unroll
    for (int j = 0; j < 16; ++j) { float d = f[j] - mean; q += d * d; }
#pragma unroll
    for (int m = 1; m < 64; m <<= 1) q += __shfl_xor(q, m);
    const float rstd = rsqrtf(q * (1.f / D) + 1e-5f);
    unsigned o[8];
#pragma unroll
    for (int j = 0; j < 16; j += 2) {
      const int c0 = (j < 8) ? lane * 8 + j : 512 + lane * 8 + (j - 8);
      float a = rbf((f[j] - mean) * rstd) * sc[c0] + sh[c0];
      float b = rbf((f[j + 1] - mean) * rstd) * sc[c0 + 1] + sh[c0 + 1];
      o[j >> 1] = pack2(a, b);
    }
    u16* hr = h + (size_t)row * D;
    *reinterpret_cast<uint4*>(hr + lane * 8) = make_uint4(o[0], o[1], o[2], o[3]);
    *reinterpret_cast<uint4*>(hr + 512 + lane * 8) = make_uint4(o[4], o[5], o[6], o[7]);
  }
}

template <int UK> DI int xswz(int row, int c) {
  if (UK == 64) return c ^ ((row >> 1) & 7);
  else return c ^ ((0x78 >> (2 * ((row >> 2) & 3))) & 3);
}
template <int PPR> DI int wperm(int k, int j) {
  if (PPR == 12) { int v = j + 2 * ((k >> 3) & 1); return v >= 12 ? v - 12 : v; }
  else return j ^ (2 * (((k >> 1) & 1) | (((k >> 3) & 1) << 1)));
}
template <int PPR> DI int wperm_inv(int k, int pos) {
  if (PPR == 12) { int v = pos - 2 * ((k >> 3) & 1); return v < 0 ? v + 12 : v; }
  else return pos ^ (2 * (((k >> 1) & 1) | (((k >> 3) & 1) << 1)));
}

template <int BM, int NB, int WM, int WN, int NST, typename ColMap>
DI void gemm_core(const u16* __restrict__ X, const u16* __restrict__ W, int ldw, int m0, ColMap colmap, char* lds,
                  f32x4 (&acc)[NB / WN][BM / WM / 16]) {
  constexpr int UK = 64;
  constexpr int MT = BM / WM / 16, NW = NB / WN;
  constexpr int XRB = UK * 2, CPR = UK / 8;
  constexpr int PPR = NB * 2, WRB = NB * 32;
  constexpr int XB = BM * XRB, WB = UK * WRB, UB = XB + WB;
  constexpr int XT = XB / 1024, WT = WB / 1024;
  constexpr int XI = XT / 8, WI = (WT + 7) / 8, WMIN = WT / 8;
  constexpr int NU = D / UK;
  static_assert(XT % 8 == 0, "");
  const int t = threadIdx.x, wid = __builtin_amdgcn_readfirstlane(t >> 6), lane = t & 63;
  const u16* xsrc[XI];
#pragma unroll
  for (int i = 0; i < XI; ++i) {
    const int p = (wid * XI + i) * 64 + lane;
    const int row = p / CPR, c = xswz<UK>(row, p % CPR);
    xsrc[i] = X + (size_t)(m0 + row) * D + c * 8;
  }
  const u16* wsrc[WI];
#pragma unroll
  for (int i = 0; i < WI; ++i) {
    const int p = (wid + i * 8) * 64 + lane;
    const int k = (p / PPR) % UK, j = wperm_inv<PPR>(k, p % PPR);
    wsrc[i] = W + (size_t)k * ldw + colmap(j >> 1) + 8 * (j & 1);
  }
  auto issue = [&](int u) {
    char* base = lds + (u % NST) * UB;
#pragma unroll
    for (int i = 0; i < XI; ++i)
      __builtin_amdgcn_global_load_lds((const unsigned*)(xsrc[i] + u * UK), (LDSP(unsigned))(base + (wid * XI + i) * 1024), 16, 0, 0);
#pragma unroll
    for (int i = 0; i < WI; ++i)
      if (wid + i * 8 < WT)
        __builtin_amdgcn_global_load_lds((const unsigned*)(wsrc[i] + (size_t)u * UK * ldw), (LDSP(unsigned))(base + XB + (wid + i * 8) * 1024), 16, 0, 0);
  };
  const int wm = wid / WN, wn = wid % WN;
  const int l15 = lane & 15, g = lane >> 4, q = l15 >> 2, p = lane & 3;
  int xoff[MT][2], woff[NW][2][2];
#pragma unroll
  for (int kk = 0; kk < 2; ++kk) {
#pragma unroll
    for (int mt = 0; mt < MT; ++mt) {
      const int row = wm * (BM / WM) + mt * 16 + l15;
      xoff[mt][kk] = row * XRB + xswz<UK>(row, kk * 4 + g) * 16;
    }
#pragma unroll
    for (int nb = 0; nb < NW; ++nb)
#pragma unroll
      for (int tt = 0; tt < 2; ++tt) {
        const int k = kk * 32 + 8 * g + 4 * tt + q;
        woff[nb][kk][tt] = XB + k * WRB + wperm<PPR>(k, 2 * (wn * NW + nb) + (p >> 1)) * 16 + 8 * (p & 1);
      }
  }
  bf16x8 fa[2][NW], fb[2][MT];
  auto rd = [&](int kk, int u) {
    const char* base = lds + (u % NST) * UB;
#pragma unroll
    for (int mt = 0; mt < MT; ++mt) fb[kk][mt] = *reinterpret_cast<const bf16x8*>(base + xoff[mt][kk]);
#pragma unroll
    for (int nb = 0; nb < NW; ++nb) {
      s16x4 a0 = __builtin_amdgcn_ds_read_tr16_b64_v4i16((LDSP(s16x4))(base + woff[nb][kk][0]));
      s16x4 a1 = __builtin_amdgcn_ds_read_tr16_b64_v4i16((LDSP(s16x4))(base + woff[nb][kk][1]));
      fa[kk][nb] = __builtin_shufflevector(a0, a1, 0, 1, 2, 3, 4, 5, 6, 7);
    }
  };
  auto mm = [&](int kk) {
#pragma unroll
    for (int nb = 0; nb < NW; ++nb)
#pragma unroll
      for (int mt = 0; mt < MT; ++mt)
        acc[nb][mt] = __builtin_amdgcn_mfma_f32_16x16x32_bf16(fa[kk][nb], fb[kk][mt], acc[nb][mt], 0, 0, 0);
  };
#pragma unroll
  for (int u = 0; u < NST - 1; ++u) issue(u);
  asm volatile("s_waitcnt vmcnt(%0)" ::"i"((NST - 2) * (XI + WMIN)) : "memory");
  __builtin_amdgcn_s_barrier();
  rd(0, 0);
  for (int u = 0; u < NU; ++u) {
    if (u + NST - 1 < NU) issue(u + NST - 1);
    rd(1, u);
    mm(0);
    if (u + NST - 1 < NU) { asm volatile("s_waitcnt vmcnt(%0) lgkmcnt(0)" ::"i"((NST - 2) * (XI + WMIN)) : "memory"); }
    else                  { asm volatile("s_waitcnt vmcnt(0) lgkmcnt(0)" ::: "memory"); }
    __builtin_amdgcn_s_barrier();
    if (u + 1 < NU) rd(0, u + 1);
    mm(1);
  }
  __syncthreads();
}

DI void lds_barrier() { asm volatile("s_waitcnt lgkmcnt(0)" ::: "memory"); __builtin_amdgcn_s_barrier(); }

DI float red_x16(float x, bool mx) {
  auto r = __builtin_amdgcn_permlane16_swap(__float_as_uint(x), __float_as_uint(x), false, false);
  float a = __uint_as_float(r[0]), b = __uint_as_float(r[1]);
  return mx ? fmaxf(a, b) : a + b;
}
DI float red_x32(float x, bool mx) {
  auto r = __builtin_amdgcn_permlane32_swap(__float_as_uint(x), __float_as_uint(x), false, false);
  float a = __uint_as_float(r[0]), b = __uint_as_float(r[1]);
  return mx ? fmaxf(a, b) : a + b;
}

DI void softmax_pm(const f32x4& p0, const f32x4& p1, float (&ep)[8], float (&en)[8]) {
  float v[8] = {rbf(p0[0]), rbf(p0[1]), rbf(p0[2]), rbf(p0[3]), rbf(p1[0]), rbf(p1[1]), rbf(p1[2]), rbf(p1[3])};
  float mx = v[0], mn = v[0];
#pragma unroll
  for (int j = 1; j < 8; ++j) { mx = fmaxf(mx, v[j]); mn = fminf(mn, v[j]); }
  float nmn = -mn;
  mx = red_x32(red_x16(mx, true), true);
  nmn = red_x32(red_x16(nmn, true), true);
  float sp = 0.f, sn = 0.f;
#pragma unroll
  for (int j = 0; j < 8; ++j) {
    ep[j] = __expf(v[j] - mx); en[j] = __expf(-nmn - v[j]);
    sp += ep[j]; sn += en[j];
  }
  sp = red_x32(red_x16(sp, false), false);
  sn = red_x32(red_x16(sn, false), false);
  const float rp = __builtin_amdgcn_rcpf(sp), rn = __builtin_amdgcn_rcpf(sn);
#pragma unroll
  for (int j = 0; j < 8; ++j) { ep[j] *= rp; en[j] *= rn; }
}

__global__ __launch_bounds__(512) void k_qkv(const u16* __restrict__ h1, const u16* __restrict__ wqkv,
                                             const u16* __restrict__ wphi, u16* __restrict__ phq,
                                             float* __restrict__ kvp, float* __restrict__ zp) {
  extern __shared__ __attribute__((aligned(16))) char lds[];
  const int b = blockIdx.x, xcd = b & 7, idx = b >> 3;
  const int mtile = 2 * (xcd & 3) + (idx & 1), head = 16 * (xcd >> 2) + (idx >> 1);
  const int m0 = mtile * 256;
  STAMP(2, 0);
  const int t = threadIdx.x, wid = t >> 6, lane = t & 63, l15 = lane & 15, g = lane >> 4;
  bf16x8 aphi[2];
#pragma unroll
  for (int ft = 0; ft < 2; ++ft)
#pragma unroll
    for (int j = 0; j < 8; ++j) aphi[ft][j] = (short)wphi[(head * 32 + 8 * g + j) * 32 + 16 * ft + l15];
  f32x4 acc[3][4];
#pragma unroll
  for (int i = 0; i < 3; ++i)
#pragma unroll
    for (int j = 0; j < 4; ++j) acc[i][j] = (f32x4){0.f, 0.f, 0.f, 0.f};
  gemm_core<256, 6, 4, 2, 3>(h1, wqkv, 3 * D, m0, [&](int nb) { return (nb >> 1) * D + head * 32 + (nb & 1) * 16; }, lds, acc);
  STAMP(2, 1);
  constexpr int QS = 208;
  char* qkv_img = lds;
  char* pk_img = lds + 256 * QS;
  {
    const int wm = wid >> 1, wn = wid & 1;
#pragma unroll
    for (int nb = 0; nb < 3; ++nb)
#pragma unroll
      for (int mt = 0; mt < 4; ++mt) {
        const int m = wm * 64 + mt * 16 + l15, n = (wn * 3 + nb) * 16 + 4 * g;
        *reinterpret_cast<uint2*>(qkv_img + m * QS + n * 2) =
            make_uint2(pack2(acc[nb][mt][0], acc[nb][mt][1]), pack2(acc[nb][mt][2], acc[nb][mt][3]));
      }
  }
  lds_barrier();
#pragma unroll
  for (int mt = 0; mt < 2; ++mt) {
    const int m = wid * 32 + mt * 16 + l15;
    const f32x4 z4 = {0.f, 0.f, 0.f, 0.f};
    {
      bf16x8 bq = *reinterpret_cast<const bf16x8*>(qkv_img + m * QS + 16 * g);
      f32x4 p0 = __builtin_amdgcn_mfma_f32_16x16x32_bf16(aphi[0], bq, z4, 0, 0, 0);
      f32x4 p1 = __builtin_amdgcn_mfma_f32_16x16x32_bf16(aphi[1], bq, z4, 0, 0, 0);
      float ep[8], en[8];
      softmax_pm(p0, p1, ep, en);
      u16* dst = phq + ((size_t)(m0 + m) * H + head) * 64 + 4 * g;
      *reinterpret_cast<uint2*>(dst)      = make_uint2(pack2(ep[0], ep[1]), pack2(ep[2], ep[3]));
      *reinterpret_cast<uint2*>(dst + 16) = make_uint2(pack2(ep[4], ep[5]), pack2(ep[6], ep[7]));
      *reinterpret_cast<uint2*>(dst + 32) = make_uint2(pack2(en[0], en[1]), pack2(en[2], en[3]));
      *reinterpret_cast<uint2*>(dst + 48) = make_uint2(pack2(en[4], en[5]), pack2(en[6], en[7]));
    }
    {
      bf16x8 bk = *reinterpret_cast<const bf16x8*>(qkv_img + m * QS + 64 + 16 * g);
      f32x4 p0 = __builtin_amdgcn_mfma_f32_16x16x32_bf16(aphi[0], bk, z4, 0, 0, 0);
      f32x4 p1 = __builtin_amdgcn_mfma_f32_16x16x32_bf16(aphi[1], bk, z4, 0, 0, 0);
      float ep[8], en[8];
      softmax_pm(p0, p1, ep, en);
      char* dst = pk_img + m * 128 + 8 * g;
      *reinterpret_cast<uint2*>(dst)      = make_uint2(pack2(ep[0], ep[1]), pack2(ep[2], ep[3]));
      *reinterpret_cast<uint2*>(dst + 32) = make_uint2(pack2(ep[4], ep[5]), pack2(ep[6], ep[7]));
      *reinterpret_cast<uint2*>(dst + 64) = make_uint2(pack2(en[0], en[1]), pack2(en[2], en[3]));
      *reinterpret_cast<uint2*>(dst + 96) = make_uint2(pack2(en[4], en[5]), pack2(en[6], en[7]));
    }
  }
  lds_barrier();
  STAMP(2, 2);
  {
    f32x4 c0 = {0.f, 0.f, 0.f, 0.f}, cz = c0;
    bf16x8 ones;
#pragma unroll
    for (int j = 0; j < 8; ++j) ones[j] = (short)0x3f80;
    const int q = l15 >> 2, p = lane & 3, ft = wid & 3, dt = wid >> 2;
#pragma unroll
    for (int ks = 0; ks < 8; ++ks) {
      const int r0 = ks * 32 + 8 * g + q;
      s16x4 a0 = __builtin_amdgcn_ds_read_tr16_b64_v4i16((LDSP(s16x4))(pk_img + (r0) * 128 + (16 * ft + 4 * p) * 2));
      s16x4 a1 = __builtin_amdgcn_ds_read_tr16_b64_v4i16((LDSP(s16x4))(pk_img + (r0 + 4) * 128 + (16 * ft + 4 * p) * 2));
      bf16x8 afr = __builtin_shufflevector(a0, a1, 0, 1, 2, 3, 4, 5, 6, 7);
      s16x4 b0 = __builtin_amdgcn_ds_read_tr16_b64_v4i16((LDSP(s16x4))(qkv_img + (r0) * QS + 128 + (16 * dt + 4 * p) * 2));
      s16x4 b1 = __builtin_amdgcn_ds_read_tr16_b64_v4i16((LDSP(s16x4))(qkv_img + (r0 + 4) * QS + 128 + (16 * dt + 4 * p) * 2));
      bf16x8 bf0 = __builtin_shufflevector(b0, b1, 0, 1, 2, 3, 4, 5, 6, 7);
      c0 = __builtin_amdgcn_mfma_f32_16x16x32_bf16(afr, bf0, c0, 0, 0, 0);
      cz = __builtin_amdgcn_mfma_f32_16x16x32_bf16(afr, ones, cz, 0, 0, 0);
    }
    float* kd = kvp + ((size_t)(mtile * H + head) * 64) * 32;
#pragma unroll
    for (int r = 0; r < 4; ++r) {
      const int f = 16 * ft + 4 * g + r;
      kd[f * 32 + 16 * dt + l15] = c0[r];
      if (l15 == 0 && dt == 0) zp[(mtile * H + head) * 64 + f] = cz[r];
    }
  }
  STAMP(2, 3);
}

__global__ __launch_bounds__(256) void k_attn_o(const u16* __restrict__ phq, const float* __restrict__ kvp,
                                                const float* __restrict__ zp, u16* __restrict__ o) {
  __shared__ __attribute__((aligned(16))) u16 kvT[32][64 + 8];
  __shared__ float zs[64];
  const int head = blockIdx.x & 31, chunk = blockIdx.x >> 5;
  const int t = threadIdx.x, wid = t >> 6, lane = t & 63, l15 = lane & 15, g = lane >> 4;
  {
    const int f = t >> 2, d0 = (t & 3) * 8;
    float s[8];
#pragma unroll
    for (int j = 0; j < 8; ++j) s[j] = 0.f;
#pragma unroll
    for (int p = 0; p < 8; ++p) {
      const float* src = kvp + ((size_t)(p * H + head) * 64 + f) * 32 + d0;
      float4 a = *reinterpret_cast<const float4*>(src);
      float4 b2 = *reinterpret_cast<const float4*>(src + 4);
      s[0] += a.x; s[1] += a.y; s[2] += a.z; s[3] += a.w;
      s[4] += b2.x; s[5] += b2.y; s[6] += b2.z; s[7] += b2.w;
    }
#pragma unroll
    for (int j = 0; j < 8; ++j) kvT[d0 + j][f] = f2bf(s[j]);
    if (t < 64) {
      float z = 0.f;
#pragma unroll
      for (int p = 0; p < 8; ++p) z += zp[(p * H + head) * 64 + t];
      zs[t] = rbf(z);
    }
  }
  __syncthreads();
  bf16x8 af[2][2];
#pragma unroll
  for (int dt = 0; dt < 2; ++dt)
#pragma unroll
    for (int kk = 0; kk < 2; ++kk)
      af[dt][kk] = *reinterpret_cast<const bf16x8*>(&kvT[16 * dt + l15][32 * kk + 8 * g]);
  float zr[2][8];
#pragma unroll
  for (int kk = 0; kk < 2; ++kk)
#pragma unroll
    for (int j = 0; j < 8; ++j) zr[kk][j] = zs[32 * kk + 8 * g + j];
#pragma unroll
  for (int mt = 0; mt < 4; ++mt) {
    const int m = chunk * 256 + wid * 64 + mt * 16 + l15;
    const u16* src = phq + ((size_t)m * H + head) * 64 + 8 * g;
    bf16x8 b0 = *reinterpret_cast<const bf16x8*>(src);
    bf16x8 b1 = *reinterpret_cast<const bf16x8*>(src + 32);
    float den = 0.f;
#pragma unroll
    for (int j = 0; j < 8; ++j) den += bf2f((u16)b0[j]) * zr[0][j] + bf2f((u16)b1[j]) * zr[1][j];
    den += __shfl_xor(den, 16); den += __shfl_xor(den, 32);
    den = rbf(rbf(den) + 1e-6f);
    const float rden = 1.f / den;
#pragma unroll
    for (int dt = 0; dt < 2; ++dt) {
      f32x4 c = {0.f, 0.f, 0.f, 0.f};
      c = __builtin_amdgcn_mfma_f32_16x16x32_bf16(af[dt][0], b0, c, 0, 0, 0);
      c = __builtin_amdgcn_mfma_f32_16x16x32_bf16(af[dt][1], b1, c, 0, 0, 0);
      *reinterpret_cast<uint2*>(o + (size_t)m * D + head * 32 + 16 * dt + 4 * g) =
          make_uint2(pack2(rbf(c[0]) * rden, rbf(c[1]) * rden), pack2(rbf(c[2]) * rden, rbf(c[3]) * rden));
    }
  }
}

__global__ __launch_bounds__(512) void k_oproj(const u16* __restrict__ o, const u16* __restrict__ wo,
                                               const u16* __restrict__ x, const float* __restrict__ modpart,
                                               u16* __restrict__ x1) {
  extern __shared__ __attribute__((aligned(16))) char lds[];
  const int b = blockIdx.x, xcd = b & 7, idx = b >> 3;
  const int mtile = 2 * xcd + (idx & 1), ntile = idx >> 1;
  const int m0 = mtile * 128, n0 = ntile * 64;
  STAMP(4, 0);
  const int t = threadIdx.x, wid = t >> 6, lane = t & 63, l15 = lane & 15, g = lane >> 4;
  f32x4 acc[2][2];
#pragma unroll
  for (int i = 0; i < 2; ++i)
#pragma unroll
    for (int j = 0; j < 2; ++j) acc[i][j] = (f32x4){0.f, 0.f, 0.f, 0.f};
  gemm_core<128, 4, 4, 2, 6>(o, wo, D, m0, [&](int nb) { return n0 + nb * 16; }, lds, acc);
  const int wm = wid >> 1, wn = wid & 1;
  STAMP(4, 1);
#pragma unroll
  for (int nb = 0; nb < 2; ++nb) {
    const int n = n0 + (wn * 2 + nb) * 16 + 4 * g;
    float ga[4];
#pragma unroll
    for (int v = 0; v < 2; ++v) {}
    {
      float4 a = *reinterpret_cast<const float4*>(modpart + 2 * D + n);
#pragma unroll
      for (int p = 1; p < 4; ++p) {
        float4 a2 = *reinterpret_cast<const float4*>(modpart + p * 6144 + 2 * D + n);
        a.x += a2.x; a.y += a2.y; a.z += a2.z; a.w += a2.w;
      }
      ga[0] = rbf(a.x); ga[1] = rbf(a.y); ga[2] = rbf(a.z); ga[3] = rbf(a.w);
    }
#pragma unroll
    for (int mt = 0; mt < 2; ++mt) {
      const int m = m0 + wm * 32 + mt * 16 + l15;
      uint2 xv = *reinterpret_cast<const uint2*>(x + (size_t)m * D + n);
      float r0 = lo16(xv.x) + rbf(ga[0] * rbf(acc[nb][mt][0]));
      float r1 = hi16(xv.x) + rbf(ga[1] * rbf(acc[nb][mt][1]));
      float r2 = lo16(xv.y) + rbf(ga[2] * rbf(acc[nb][mt][2]));
      float r3 = hi16(xv.y) + rbf(ga[3] * rbf(acc[nb][mt][3]));
      *reinterpret_cast<uint2*>(x1 + (size_t)m * D + n) = make_uint2(pack2(r0, r1), pack2(r2, r3));
    }
  }
  STAMP(4, 2);
}

#define RLX_AGENT __ATOMIC_RELAXED, __HIP_MEMORY_SCOPE_AGENT
DI void st_agent(float* p, float v) { __hip_atomic_store(p, v, RLX_AGENT); }
DI float ld_agent(const float* p) { return __hip_atomic_load(p, RLX_AGENT); }
DI void signal(unsigned* ctr) {
  asm volatile("s_waitcnt vmcnt(0)" ::: "memory");
  __syncthreads();
  if (threadIdx.x == 0) __hip_atomic_fetch_add(ctr, 1u, RLX_AGENT);
}
DI void wait_ctr(unsigned* ctr, unsigned target) {
  if (threadIdx.x == 0) {
    for (unsigned spins = 0; spins < 4000000u; ++spins) {
      if (__hip_atomic_load(ctr, RLX_AGENT) >= target) break;
      __builtin_amdgcn_s_sleep(1);
    }
  }
  __syncthreads();
}

DI void ln_row(const u16* xr, int lane, float (&f)[16], float& mean, float& rstd) {
  uint4 v0 = *reinterpret_cast<const uint4*>(xr + lane * 8);
  uint4 v1 = *reinterpret_cast<const uint4*>(xr + 512 + lane * 8);
  f[0] = lo16(v0.x); f[1] = hi16(v0.x); f[2] = lo16(v0.y); f[3] = hi16(v0.y);
  f[4] = lo16(v0.z); f[5] = hi16(v0.z); f[6] = lo16(v0.w); f[7] = hi16(v0.w);
  f[8] = lo16(v1.x); f[9] = hi16(v1.x); f[10] = lo16(v1.y); f[11] = hi16(v1.y);
  f[12] = lo16(v1.z); f[13] = hi16(v1.z); f[14] = lo16(v1.w); f[15] = hi16(v1.w);
  float s = 0.f;
#pragma unroll
  for (int j = 0; j < 16; ++j) s += f[j];
#pragma unroll
  for (int m = 1; m < 64; m <<= 1) s += __shfl_xor(s, m);
  mean = s * (1.f / D);
  float q = 0.f;
#pragma unroll
  for (int j = 0; j < 16; ++j) { float d = f[j] - mean; q += d * d; }
#pragma unroll
  for (int m = 1; m < 64; m <<= 1) q += __shfl_xor(q, m);
  rstd = rsqrtf(q * (1.f / D) + 1e-5f);
}

__global__ __launch_bounds__(256) void k_tail(const u16* __restrict__ x1, const float* __restrict__ modpart,
                                              const u16* __restrict__ wr, const u16* __restrict__ wg,
                                              const u16* __restrict__ wu, const u16* __restrict__ wd,
                                              u16* __restrict__ out, float* __restrict__ r, float* __restrict__ gup,
                                              float* __restrict__ dpart, unsigned* __restrict__ ctr) {
  __shared__ float sc[D], sh[D], gbv[D];
  __shared__ float red[32][65];
  __shared__ float hsel[2][D];
  __shared__ float hs[2][256];
  __shared__ float sm_v[8]; __shared__ int sm_i[8];
  const int b = blockIdx.x, t = threadIdx.x, wid = t >> 6, lane = t & 63;
  const int cq = t & 7, rr = t >> 3;
  const int gcc = b % 56, gks = b / 56;
  uint4 wa[8], wb[8];
  if (b < 224) {
#pragma unroll
    for (int i = 0; i < 8; ++i) {
      const int k = gks * 256 + i * 32 + rr;
      wa[i] = *reinterpret_cast<const uint4*>(wg + (size_t)k * F + gcc * 64 + cq * 8);
      wb[i] = *reinterpret_cast<const uint4*>(wu + (size_t)k * F + gcc * 64 + cq * 8);
    }
  }
  {
    const int c4 = t * 4;
    float4 a = *reinterpret_cast<const float4*>(modpart + 3 * D + c4);
    float4 bb = *reinterpret_cast<const float4*>(modpart + 4 * D + c4);
    float4 c = *reinterpret_cast<const float4*>(modpart + 5 * D + c4);
#pragma unroll
    for (int p = 1; p < 4; ++p) {
      float4 a2 = *reinterpret_cast<const float4*>(modpart + p * 6144 + 3 * D + c4);
      float4 b2 = *reinterpret_cast<const float4*>(modpart + p * 6144 + 4 * D + c4);
      float4 c2 = *reinterpret_cast<const float4*>(modpart + p * 6144 + 5 * D + c4);
      a.x += a2.x; a.y += a2.y; a.z += a2.z; a.w += a2.w;
      bb.x += b2.x; bb.y += b2.y; bb.z += b2.z; bb.w += b2.w;
      c.x += c2.x; c.y += c2.y; c.z += c2.z; c.w += c2.w;
    }
    sh[c4] = rbf(a.x); sh[c4 + 1] = rbf(a.y); sh[c4 + 2] = rbf(a.z); sh[c4 + 3] = rbf(a.w);
    sc[c4] = rbf(1.f + rbf(bb.x)); sc[c4 + 1] = rbf(1.f + rbf(bb.y));
    sc[c4 + 2] = rbf(1.f + rbf(bb.z)); sc[c4 + 3] = rbf(1.f + rbf(bb.w));
    gbv[c4] = rbf(c.x); gbv[c4 + 1] = rbf(c.y); gbv[c4 + 2] = rbf(c.z); gbv[c4 + 3] = rbf(c.w);
  }
  __syncthreads();
  {
    uint4 w0 = *reinterpret_cast<const uint4*>(wr + lane * 8);
    uint4 w1 = *reinterpret_cast<const uint4*>(wr + 512 + lane * 8);
    float wf[16];
    wf[0] = lo16(w0.x); wf[1] = hi16(w0.x); wf[2] = lo16(w0.y); wf[3] = hi16(w0.y);
    wf[4] = lo16(w0.z); wf[5] = hi16(w0.z); wf[6] = lo16(w0.w); wf[7] = hi16(w0.w);
    wf[8] = lo16(w1.x); wf[9] = hi16(w1.x); wf[10] = lo16(w1.y); wf[11] = hi16(w1.y);
    wf[12] = lo16(w1.z); wf[13] = hi16(w1.z); wf[14] = lo16(w1.w); wf[15] = hi16(w1.w);
#pragma unroll
    for (int q2 = 0; q2 < 2; ++q2) {
      const int row = b * 8 + wid * 2 + q2;
      float f[16], mean, rstd;
      ln_row(x1 + (size_t)row * D, lane, f, mean, rstd);
      unsigned oo[8];
      float dot = 0.f;
#pragma unroll
      for (int j = 0; j < 16; j += 2) {
        const int c0 = (j < 8) ? lane * 8 + j : 512 + lane * 8 + (j - 8);
        float a = rbf(rbf((f[j] - mean) * rstd) * sc[c0] + sh[c0]);
        float bq = rbf(rbf((f[j + 1] - mean) * rstd) * sc[c0 + 1] + sh[c0 + 1]);
        dot += a * wf[j] + bq * wf[j + 1];
        oo[j >> 1] = pack2(f[j] + rbf(gbv[c0] * a), f[j + 1] + rbf(gbv[c0 + 1] * bq));
      }
#pragma unroll
      for (int m = 1; m < 64; m <<= 1) dot += __shfl_xor(dot, m);
      u16* orow = out + (size_t)row * D;
      *reinterpret_cast<uint4*>(orow + lane * 8) = make_uint4(oo[0], oo[1], oo[2], oo[3]);
      *reinterpret_cast<uint4*>(orow + 512 + lane * 8) = make_uint4(oo[4], oo[5], oo[6], oo[7]);
      if (lane == 0) st_agent(r + row, rbf(dot));
    }
  }
  signal(ctr + 0);
  wait_ctr(ctr + 0, 256);
  int i0, i1; float r0, r1;
  {
    float b0 = -INFINITY, b1 = -INFINITY; int j0 = 0x7fffffff, j1 = 0x7fffffff;
#pragma unroll
    for (int i = 0; i < 8; ++i) {
      const int idx = t + i * 256;
      const float v = ld_agent(r + idx);
      if (v > b0 || (v == b0 && idx < j0)) { b1 = b0; j1 = j0; b0 = v; j0 = idx; }
      else if (v > b1 || (v == b1 && idx < j1)) { b1 = v; j1 = idx; }
    }
#pragma unroll
    for (int m = 1; m < 64; m <<= 1) {
      float o0 = __shfl_xor(b0, m), o1 = __shfl_xor(b1, m);
      int p0 = __shfl_xor(j0, m), p1 = __shfl_xor(j1, m);
      if (o0 > b0 || (o0 == b0 && p0 < j0)) {
        if (b0 > o1 || (b0 == o1 && j0 < p1)) { b1 = b0; j1 = j0; } else { b1 = o1; j1 = p1; }
        b0 = o0; j0 = p0;
      } else {
        if (o0 > b1 || (o0 == b1 && p0 < j1)) { b1 = o0; j1 = p0; }
      }
    }
    if (lane == 0) { sm_v[wid * 2] = b0; sm_v[wid * 2 + 1] = b1; sm_i[wid * 2] = j0; sm_i[wid * 2 + 1] = j1; }
    __syncthreads();
    b0 = sm_v[0]; b1 = sm_v[1]; j0 = sm_i[0]; j1 = sm_i[1];
#pragma unroll
    for (int w = 1; w < 4; ++w) {
      float o0 = sm_v[w * 2], o1 = sm_v[w * 2 + 1];
      int p0 = sm_i[w * 2], p1 = sm_i[w * 2 + 1];
      if (o0 > b0 || (o0 == b0 && p0 < j0)) {
        if (b0 > o1 || (b0 == o1 && j0 < p1)) { b1 = b0; j1 = j0; } else { b1 = o1; j1 = p1; }
        b0 = o0; j0 = p0;
      } else {
        if (o0 > b1 || (o0 == b1 && p0 < j1)) { b1 = o0; j1 = p0; }
      }
    }
    i0 = j0; i1 = j1; r0 = b0; r1 = b1;
  }
  if (wid < 2) {
    const int row = wid ? i1 : i0;
    float f[16], mean, rstd;
    ln_row(x1 + (size_t)row * D, lane, f, mean, rstd);
#pragma unroll
    for (int j = 0; j < 16; ++j) {
      const int c0 = (j < 8) ? lane * 8 + j : 512 + lane * 8 + (j - 8);
      hsel[wid][c0] = rbf(rbf((f[j] - mean) * rstd) * sc[c0] + sh[c0]);
    }
  }
  __syncthreads();
  const int dcc = b & 15, dks = b >> 4;
  uint4 wdv[8];
  if (b < 224) {
    float acc[4][8];
#pragma unroll
    for (int a = 0; a < 4; ++a)
#pragma unroll
      for (int j = 0; j < 8; ++j) acc[a][j] = 0.f;
#pragma unroll
    for (int i = 0; i < 8; ++i) {
      const int k = gks * 256 + i * 32 + rr;
      const float h0 = hsel[0][k], h1v = hsel[1][k];
      const unsigned wv[4] = {wa[i].x, wa[i].y, wa[i].z, wa[i].w};
      const unsigned uv[4] = {wb[i].x, wb[i].y, wb[i].z, wb[i].w};
#pragma unroll
      for (int j = 0; j < 4; ++j) {
        acc[0][2 * j] += h0 * lo16(wv[j]);  acc[0][2 * j + 1] += h0 * hi16(wv[j]);
        acc[1][2 * j] += h1v * lo16(wv[j]); acc[1][2 * j + 1] += h1v * hi16(wv[j]);
        acc[2][2 * j] += h0 * lo16(uv[j]);  acc[2][2 * j + 1] += h0 * hi16(uv[j]);
        acc[3][2 * j] += h1v * lo16(uv[j]); acc[3][2 * j + 1] += h1v * hi16(uv[j]);
      }
    }
#pragma unroll
    for (int i = 0; i < 8; ++i) {
      const int k = dks * 256 + i * 32 + rr;
      wdv[i] = *reinterpret_cast<const uint4*>(wd + (size_t)k * D + dcc * 64 + cq * 8);
    }
#pragma unroll
    for (int a = 0; a < 4; ++a) {
      __syncthreads();
#pragma unroll
      for (int j = 0; j < 8; ++j) red[rr][cq * 8 + j] = acc[a][j];
      __syncthreads();
      if (t < 64) {
        float sum = 0.f;
#pragma unroll
        for (int i = 0; i < 32; ++i) sum += red[i][t];
        st_agent(gup + (size_t)(gks * 4 + a) * F + gcc * 64 + t, sum);
      }
    }
    signal(ctr + 16 + (gcc >> 2));
    wait_ctr(ctr + 16 + dks, 16);
    {
      const int k = dks * 256 + t;
#pragma unroll
      for (int tok = 0; tok < 2; ++tok) {
        float gsum = 0.f, usum = 0.f;
#pragma unroll
        for (int p = 0; p < 4; ++p) {
          gsum += ld_agent(gup + (size_t)(p * 4 + tok) * F + k);
          usum += ld_agent(gup + (size_t)(p * 4 + 2 + tok) * F + k);
        }
        gsum = rbf(gsum); usum = rbf(usum);
        const float sg = rbf(gsum / (1.f + __expf(-gsum)));
        hs[tok][t] = rbf(sg * usum);
      }
    }
    __syncthreads();
    float ac2[2][8];
#pragma unroll
    for (int a = 0; a < 2; ++a)
#pragma unroll
      for (int j = 0; j < 8; ++j) ac2[a][j] = 0.f;
#pragma unroll
    for (int i = 0; i < 8; ++i) {
      const int kl = i * 32 + rr;
      const float h0 = hs[0][kl], h1v = hs[1][kl];
      const unsigned wv[4] = {wdv[i].x, wdv[i].y, wdv[i].z, wdv[i].w};
#pragma unroll
      for (int j = 0; j < 4; ++j) {
        ac2[0][2 * j] += h0 * lo16(wv[j]);  ac2[0][2 * j + 1] += h0 * hi16(wv[j]);
        ac2[1][2 * j] += h1v * lo16(wv[j]); ac2[1][2 * j + 1] += h1v * hi16(wv[j]);
      }
    }
#pragma unroll
    for (int a = 0; a < 2; ++a) {
      __syncthreads();
#pragma unroll
      for (int j = 0; j < 8; ++j) red[rr][cq * 8 + j] = ac2[a][j];
      __syncthreads();
      if (t < 64) {
        float sum = 0.f;
#pragma unroll
        for (int i = 0; i < 32; ++i) sum += red[i][t];
        st_agent(dpart + (size_t)(dks * 2 + a) * D + dcc * 64 + t, sum);
      }
    }
    signal(ctr + 1);
  }
  const bool own0 = (i0 >> 3) == b, own1 = (i1 >> 3) == b;
  if (own0 || own1) {
    wait_ctr(ctr + 1, 224);
#pragma unroll
    for (int tok = 0; tok < 2; ++tok) {
      if (!(tok ? own1 : own0)) continue;
      const int row = tok ? i1 : i0;
      const float rv = tok ? r1 : r0;
      const float sig = rbf(1.f / (1.f + __expf(-rv)));
#pragma unroll
      for (int q4 = 0; q4 < 4; ++q4) {
        const int n = q4 * 256 + t;
        float y = 0.f;
#pragma unroll
        for (int p = 0; p < 14; ++p) y += ld_agent(dpart + (size_t)(p * 2 + tok) * D + n);
        y = rbf(rbf(y) * sig);
        const float hv = hsel[tok][n];
        const float xv = bf2f(x1[(size_t)row * D + n]);
        out[(size_t)row * D + n] = f2bf(xv + rbf(gbv[n] * rbf(hv + y)));
      }
    }
  }
}

extern "C" void kernel_launch(void* const* d_in, const int* in_sizes, int n_in, void* d_out, int out_size,
                              void* d_ws, size_t ws_size, hipStream_t stream) {
  const u16* x    = (const u16*)d_in[0];
  const u16* wqkv = (const u16*)d_in[1];
  const u16* wphi = (const u16*)d_in[2];
  const u16* wo   = (const u16*)d_in[3];
  const u16* w_r  = (const u16*)d_in[5];
  const u16* wg   = (const u16*)d_in[6];
  const u16* wu   = (const u16*)d_in[7];
  const u16* wd   = (const u16*)d_in[8];
  const u16* c    = (const u16*)d_in[9];
  const u16* wmod = (const u16*)d_in[10];
  u16* out = (u16*)d_out;

  char* ws = (char*)d_ws;
  size_t off = 0;
  auto alloc = [&](size_t bytes) { char* p = ws + off; off += (bytes + 255) & ~(size_t)255; return p; };
  float* modpart = (float*)alloc(4 * 6144 * 4);
  u16* h1  = (u16*)alloc((size_t)S * D * 2);
  u16* phq = (u16*)alloc((size_t)S * H * 64 * 2);
  float* kvp = (float*)alloc((size_t)8 * H * 64 * 32 * 4);
  float* zp  = (float*)alloc((size_t)8 * H * 64 * 4);
  u16* o   = (u16*)alloc((size_t)S * D * 2);
  u16* x1  = (u16*)alloc((size_t)S * D * 2);
  float* r = (float*)alloc(S * 4);
  float* gup = (float*)alloc((size_t)16 * F * 4);
  float* dpart = (float*)alloc((size_t)28 * D * 4);
  unsigned* ctr = (unsigned*)alloc(256);

  k_mod<<<384, 256, 0, stream>>>(c, wmod, modpart, ctr);
  k_ln1<<<256, 256, 0, stream>>>(x, modpart, h1);
  constexpr int LDS_QKV = 3 * (256 * 128 + 64 * 192);
  constexpr int LDS_OP  = 6 * (128 * 128 + 64 * 128);
  hipFuncSetAttribute((const void*)k_qkv, hipFuncAttributeMaxDynamicSharedMemorySize, LDS_QKV);
  hipFuncSetAttribute((const void*)k_oproj, hipFuncAttributeMaxDynamicSharedMemorySize, LDS_OP);
  k_qkv<<<256, 512, LDS_QKV, stream>>>(h1, wqkv, wphi, phq, kvp, zp);
  k_attn_o<<<256, 256, 0, stream>>>(phq, kvp, zp, o);
  k_oproj<<<256, 512, LDS_OP, stream>>>(o, wo, x, modpart, x1);
  k_tail<<<256, 256, 0, stream>>>(x1, modpart, w_r, wg, wu, wd, out, r, gup, dpart, ctr);
}
```

```cpp
#include <hip/hip_runtime.h>
#include <hip/hip_bf16.h>
#include <stdint.h>
#define OPST 3

constexpr int S  = 2048;
constexpr int D  = 1024;
constexpr int H  = 32;
constexpr int F  = 3584;

typedef short  s16x4  __attribute__((ext_vector_type(4)));
typedef short  bf16x8 __attribute__((ext_vector_type(8)));
typedef float  f32x4  __attribute__((ext_vector_type(4)));
typedef unsigned short u16;
#define LDSP(T) __attribute__((address_space(3))) T*
#define DI __device__ __forceinline__
#define STAMP(k, i) do {} while (0)

DI float bf2f(u16 x) { return __uint_as_float(((unsigned)x) << 16); }
typedef __bf16 bf16v2 __attribute__((ext_vector_type(2)));
typedef float f32v2 __attribute__((ext_vector_type(2)));
DI u16 f2bf(float f) { __bf16 b = (__bf16)f; return __builtin_bit_cast(u16, b); }
DI float rbf(float f) { return (float)(__bf16)f; }
DI unsigned pack2(float a, float b) {
  f32v2 v = {a, b};
  return __builtin_bit_cast(unsigned, __builtin_convertvector(v, bf16v2));
}
DI float lo16(unsigned u) { return __uint_as_float(u << 16); }
DI float hi16(unsigned u) { return __uint_as_float(u & 0xffff0000u); }

__global__ __launch_bounds__(256) void k_mod(const u16* __restrict__ c, const u16* __restrict__ wmod,
                                             float* __restrict__ part, unsigned* __restrict__ ctr) {
  if (blockIdx.x == 0 && threadIdx.x < 64) ctr[threadIdx.x] = 0u;
  __shared__ float red[32][65];
  const int cc = blockIdx.x % 96, ks = blockIdx.x / 96;
  const int t = threadIdx.x, cq = t & 7, rr = t >> 3;
  const int col0 = cc * 64 + cq * 8;
  uint4 w[8];
#pragma unroll
  for (int i = 0; i < 8; ++i) {
    const int k = ks * 256 + i * 32 + rr;
    w[i] = *reinterpret_cast<const uint4*>(wmod + (size_t)k * 6144 + col0);
  }
  float acc[8];
#pragma unroll
  for (int j = 0; j < 8; ++j) acc[j] = 0.f;
#pragma unroll
  for (int i = 0; i < 8; ++i) {
    const int k = ks * 256 + i * 32 + rr;
    const float cv = bf2f(c[k]);
    const float s = rbf(cv / (1.f + __expf(-cv)));
    acc[0] += s * lo16(w[i].x); acc[1] += s * hi16(w[i].x);
    acc[2] += s * lo16(w[i].y); acc[3] += s * hi16(w[i].y);
    acc[4] += s * lo16(w[i].z); acc[5] += s * hi16(w[i].z);
    acc[6] += s * lo16(w[i].w); acc[7] += s * hi16(w[i].w);
  }
#pragma unroll
  for (int j = 0; j < 8; ++j) red[rr][cq * 8 + j] = acc[j];
  __syncthreads();
  if (t < 64) {
    float s = 0.f;
#pragma unroll
    for (int i = 0; i < 32; ++i) s += red[i][t];
    part[ks * 6144 + cc * 64 + t] = s;
  }
}

__global__ __launch_bounds__(256) void k_ln1(const u16* __restrict__ x, const float* __restrict__ modpart,
                                             u16* __restrict__ h) {
  __shared__ float sc[D], sh[D];
  const int t = threadIdx.x;
  {
    const int c4 = t * 4;
    float4 a = *reinterpret_cast<const float4*>(modpart + c4);
    float4 b = *reinterpret_cast<const float4*>(modpart + D + c4);
#pragma unroll
    for (int p = 1; p < 4; ++p) {
      float4 a2 = *reinterpret_cast<const float4*>(modpart + p * 6144 + c4);
      float4 b2 = *reinterpret_cast<const float4*>(modpart + p * 6144 + D + c4);
      a.x += a2.x; a.y += a2.y; a.z += a2.z; a.w += a2.w;
      b.x += b2.x; b.y += b2.y; b.z += b2.z; b.w += b2.w;
    }
    sh[c4] = rbf(a.x); sh[c4 + 1] = rbf(a.y); sh[c4 + 2] = rbf(a.z); sh[c4 + 3] = rbf(a.w);
    sc[c4] = rbf(1.f + rbf(b.x)); sc[c4 + 1] = rbf(1.f + rbf(b.y));
    sc[c4 + 2] = rbf(1.f + rbf(b.z)); sc[c4 + 3] = rbf(1.f + rbf(b.w));
  }
  __syncthreads();
  const int wid = t >> 6, lane = t & 63;
#pragma unroll
  for (int rr = 0; rr < 2; ++rr) {
    const int row = blockIdx.x * 8 + wid * 2 + rr;
    const u16* xr = x + (size_t)row * D;
    uint4 v0 = *reinterpret_cast<const uint4*>(xr + lane * 8);
    uint4 v1 = *reinterpret_cast<const uint4*>(xr + 512 + lane * 8);
    float f[16];
    f[0] = lo16(v0.x); f[1] = hi16(v0.x); f[2] = lo16(v0.y); f[3] = hi16(v0.y);
    f[4] = lo16(v0.z); f[5] = hi16(v0.z); f[6] = lo16(v0.w); f[7] = hi16(v0.w);
    f[8] = lo16(v1.x); f[9] = hi16(v1.x); f[10] = lo16(v1.y); f[11] = hi16(v1.y);
    f[12] = lo16(v1.z); f[13] = hi16(v1.z); f[14] = lo16(v1.w); f[15] = hi16(v1.w);
    float s = 0.f;
#pragma unroll
    for (int j = 0; j < 16; ++j) s += f[j];
#pragma unroll
    for (int m = 1; m < 64; m <<= 1) s += __shfl_xor(s, m);
    const float mean = s * (1.f / D);
    float q = 0.f;
#pragma unroll
    for (int j = 0; j < 16; ++j) { float d = f[j] - mean; q += d * d; }
#pragma unroll
    for (int m = 1; m < 64; m <<= 1) q += __shfl_xor(q, m);
    const float rstd = rsqrtf(q * (1.f / D) + 1e-5f);
    unsigned o[8];
#pragma unroll
    for (int j = 0; j < 16; j += 2) {
      const int c0 = (j < 8) ? lane * 8 + j : 512 + lane * 8 + (j - 8);
      float a = rbf((f[j] - mean) * rstd) * sc[c0] + sh[c0];
      float b = rbf((f[j + 1] - mean) * rstd) * sc[c0 + 1] + sh[c0 + 1];
      o[j >> 1] = pack2(a, b);
    }
    u16* hr = h + (size_t)row * D;
    *reinterpret_cast<uint4*>(hr + lane * 8) = make_uint4(o[0], o[1], o[2], o[3]);
    *reinterpret_cast<uint4*>(hr + 512 + lane * 8) = make_uint4(o[4], o[5], o[6], o[7]);
  }
}

template <int UK> DI int xswz(int row, int c) {
  if (UK == 64) return c ^ ((row >> 1) & 7);
  else return c ^ ((0x78 >> (2 * ((row >> 2) & 3))) & 3);
}
template <int PPR> DI int wperm(int k, int j) {
  if (PPR == 12) { int v = j + 2 * ((k >> 3) & 1); return v >= 12 ? v - 12 : v; }
  else return j ^ (2 * (((k >> 1) & 1) | (((k >> 3) & 1) << 1)));
}
template <int PPR> DI int wperm_inv(int k, int pos) {
  if (PPR == 12) { int v = pos - 2 * ((k >> 3) & 1); return v < 0 ? v + 12 : v; }
  else return pos ^ (2 * (((k >> 1) & 1) | (((k >> 3) & 1) << 1)));
}

template <int BM, int NB, int WM, int WN, int NST, typename ColMap>
DI void gemm_core(const u16* __restrict__ X, const u16* __restrict__ W, int ldw, int m0, ColMap colmap, char* lds,
                  f32x4 (&acc)[NB / WN][BM / WM / 16]) {
  constexpr int UK = 64;
  constexpr int MT = BM / WM / 16, NW = NB / WN;
  constexpr int XRB = UK * 2, CPR = UK / 8;
  constexpr int PPR = NB * 2, WRB = NB * 32;
  constexpr int XB = BM * XRB, WB = UK * WRB, UB = XB + WB;
  constexpr int XT = XB / 1024, WT = WB / 1024;
  constexpr int XI = XT / 8, WI = (WT + 7) / 8, WMIN = WT / 8;
  constexpr int NU = D / UK;
  static_assert(XT % 8 == 0, "");
  const int t = threadIdx.x, wid = __builtin_amdgcn_readfirstlane(t >> 6), lane = t & 63;
  const u16* xsrc[XI];
#pragma unroll
  for (int i = 0; i < XI; ++i) {
    const int p = (wid * XI + i) * 64 + lane;
    const int row = p / CPR, c = xswz<UK>(row, p % CPR);
    xsrc[i] = X + (size_t)(m0 + row) * D + c * 8;
  }
  const u16* wsrc[WI];
#pragma unroll
  for (int i = 0; i < WI; ++i) {
    const int p = (wid + i * 8) * 64 + lane;
    const int k = (p / PPR) % UK, j = wperm_inv<PPR>(k, p % PPR);
    wsrc[i] = W + (size_t)k * ldw + colmap(j >> 1) + 8 * (j & 1);
  }
  auto issue = [&](int u) {
    char* base = lds + (u % NST) * UB;
#pragma unroll
    for (int i = 0; i < XI; ++i)
      __builtin_amdgcn_global_load_lds((const unsigned*)(xsrc[i] + u * UK), (LDSP(unsigned))(base + (wid * XI + i) * 1024), 16, 0, 0);
#pragma unroll
    for (int i = 0; i < WI; ++i)
      if (wid + i * 8 < WT)
        __builtin_amdgcn_global_load_lds((const unsigned*)(wsrc[i] + (size_t)u * UK * ldw), (LDSP(unsigned))(base + XB + (wid + i * 8) * 1024), 16, 0, 0);
  };
  const int wm = wid / WN, wn = wid % WN;
  const int l15 = lane & 15, g = lane >> 4, q = l15 >> 2, p = lane & 3;
  int xoff[MT][2], woff[NW][2][2];
#pragma unroll
  for (int kk = 0; kk < 2; ++kk) {
#pragma unroll
    for (int mt = 0; mt < MT; ++mt) {
      const int row = wm * (BM / WM) + mt * 16 + l15;
      xoff[mt][kk] = row * XRB + xswz<UK>(row, kk * 4 + g) * 16;
    }
#pragma unroll
    for (int nb = 0; nb < NW; ++nb)
#pragma unroll
      for (int tt = 0; tt < 2; ++tt) {
        const int k = kk * 32 + 8 * g + 4 * tt + q;
        woff[nb][kk][tt] = XB + k * WRB + wperm<PPR>(k, 2 * (wn * NW + nb) + (p >> 1)) * 16 + 8 * (p & 1);
      }
  }
  bf16x8 fa[2][NW], fb[2][MT];
  auto rd = [&](int kk, int u) {
    const char* base = lds + (u % NST) * UB;
#pragma unroll
    for (int mt = 0; mt < MT; ++mt) fb[kk][mt] = *reinterpret_cast<const bf16x8*>(base + xoff[mt][kk]);
#pragma unroll
    for (int nb = 0; nb < NW; ++nb) {
      s16x4 a0 = __builtin_amdgcn_ds_read_tr16_b64_v4i16((LDSP(s16x4))(base + woff[nb][kk][0]));
      s16x4 a1 = __builtin_amdgcn_ds_read_tr16_b64_v4i16((LDSP(s16x4))(base + woff[nb][kk][1]));
      fa[kk][nb] = __builtin_shufflevector(a0, a1, 0, 1, 2, 3, 4, 5, 6, 7);
    }
  };
  auto mm = [&](int kk) {
#pragma unroll
    for (int nb = 0; nb < NW; ++nb)
#pragma unroll
      for (int mt = 0; mt < MT; ++mt)
        acc[nb][mt] = __builtin_amdgcn_mfma_f32_16x16x32_bf16(fa[kk][nb], fb[kk][mt], acc[nb][mt], 0, 0, 0);
  };
#pragma unroll
  for (int u = 0; u < NST - 1; ++u) issue(u);
  asm volatile("s_waitcnt vmcnt(%0)" ::"i"((NST - 2) * (XI + WMIN)) : "memory");
  __builtin_amdgcn_s_barrier();
  rd(0, 0);
  for (int u = 0; u < NU; ++u) {
    if (u + NST - 1 < NU) issue(u + NST - 1);
    rd(1, u);
    mm(0);
    if (u + NST - 1 < NU) { asm volatile("s_waitcnt vmcnt(%0) lgkmcnt(0)" ::"i"((NST - 2) * (XI + WMIN)) : "memory"); }
    else                  { asm volatile("s_waitcnt vmcnt(0) lgkmcnt(0)" ::: "memory"); }
    __builtin_amdgcn_s_barrier();
    if (u + 1 < NU) rd(0, u + 1);
    mm(1);
  }
  __syncthreads();
}

DI void lds_barrier() { asm volatile("s_waitcnt lgkmcnt(0)" ::: "memory"); __builtin_amdgcn_s_barrier(); }

DI float red_x16(float x, bool mx) {
  auto r = __builtin_amdgcn_permlane16_swap(__float_as_uint(x), __float_as_uint(x), false, false);
  float a = __uint_as_float(r[0]), b = __uint_as_float(r[1]);
  return mx ? fmaxf(a, b) : a + b;
}
DI float red_x32(float x, bool mx) {
  auto r = __builtin_amdgcn_permlane32_swap(__float_as_uint(x), __float_as_uint(x), false, false);
  float a = __uint_as_float(r[0]), b = __uint_as_float(r[1]);
  return mx ? fmaxf(a, b) : a + b;
}

DI void softmax_pm(const f32x4& p0, const f32x4& p1, float (&ep)[8], float (&en)[8]) {
  float v[8] = {rbf(p0[0]), rbf(p0[1]), rbf(p0[2]), rbf(p0[3]), rbf(p1[0]), rbf(p1[1]), rbf(p1[2]), rbf(p1[3])};
  float mx = v[0], mn = v[0];
#pragma unroll
  for (int j = 1; j < 8; ++j) { mx = fmaxf(mx, v[j]); mn = fminf(mn, v[j]); }
  float nmn = -mn;
  mx = red_x32(red_x16(mx, true), true);
  nmn = red_x32(red_x16(nmn, true), true);
  float sp = 0.f, sn = 0.f;
#pragma unroll
  for (int j = 0; j < 8; ++j) {
    ep[j] = __expf(v[j] - mx); en[j] = __expf(-nmn - v[j]);
    sp += ep[j]; sn += en[j];
  }
  sp = red_x32(red_x16(sp, false), false);
  sn = red_x32(red_x16(sn, false), false);
  const float rp = __builtin_amdgcn_rcpf(sp), rn = __builtin_amdgcn_rcpf(sn);
#pragma unroll
  for (int j = 0; j < 8; ++j) { ep[j] *= rp; en[j] *= rn; }
}

__global__ __launch_bounds__(512) void k_qkv(const u16* __restrict__ h1, const u16* __restrict__ wqkv,
                                             const u16* __restrict__ wphi, u16* __restrict__ phq,
                                             float* __restrict__ kvp, float* __restrict__ zp) {
  extern __shared__ __attribute__((aligned(16))) char lds[];
  const int b = blockIdx.x, xcd = b & 7, idx = b >> 3;
  const int mtile = 2 * (xcd & 3) + (idx & 1), head = 16 * (xcd >> 2) + (idx >> 1);
  const int m0 = mtile * 256;
  STAMP(2, 0);
  const int t = threadIdx.x, wid = t >> 6, lane = t & 63, l15 = lane & 15, g = lane >> 4;
  bf16x8 aphi[2];
#pragma unroll
  for (int ft = 0; ft < 2; ++ft)
#pragma unroll
    for (int j = 0; j < 8; ++j) aphi[ft][j] = (short)wphi[(head * 32 + 8 * g + j) * 32 + 16 * ft + l15];
  f32x4 acc[3][4];
#pragma unroll
  for (int i = 0; i < 3; ++i)
#pragma unroll
    for (int j = 0; j < 4; ++j) acc[i][j] = (f32x4){0.f, 0.f, 0.f, 0.f};
  gemm_core<256, 6, 4, 2, 3>(h1, wqkv, 3 * D, m0, [&](int nb) { return (nb >> 1) * D + head * 32 + (nb & 1) * 16; }, lds, acc);
  STAMP(2, 1);
  constexpr int QS = 208;
  char* qkv_img = lds;
  char* pk_img = lds + 256 * QS;
  {
    const int wm = wid >> 1, wn = wid & 1;
#pragma unroll
    for (int nb = 0; nb < 3; ++nb)
#pragma unroll
      for (int mt = 0; mt < 4; ++mt) {
        const int m = wm * 64 + mt * 16 + l15, n = (wn * 3 + nb) * 16 + 4 * g;
        *reinterpret_cast<uint2*>(qkv_img + m * QS + n * 2) =
            make_uint2(pack2(acc[nb][mt][0], acc[nb][mt][1]), pack2(acc[nb][mt][2], acc[nb][mt][3]));
      }
  }
  lds_barrier();
#pragma unroll
  for (int mt = 0; mt < 2; ++mt) {
    const int m = wid * 32 + mt * 16 + l15;
    const f32x4 z4 = {0.f, 0.f, 0.f, 0.f};
    {
      bf16x8 bq = *reinterpret_cast<const bf16x8*>(qkv_img + m * QS + 16 * g);
      f32x4 p0 = __builtin_amdgcn_mfma_f32_16x16x32_bf16(aphi[0], bq, z4, 0, 0, 0);
      f32x4 p1 = __builtin_amdgcn_mfma_f32_16x16x32_bf16(aphi[1], bq, z4, 0, 0, 0);
      float ep[8], en[8];
      softmax_pm(p0, p1, ep, en);
      u16* dst = phq + ((size_t)(m0 + m) * H + head) * 64 + 4 * g;
      *reinterpret_cast<uint2*>(dst)      = make_uint2(pack2(ep[0], ep[1]), pack2(ep[2], ep[3]));
      *reinterpret_cast<uint2*>(dst + 16) = make_uint2(pack2(ep[4], ep[5]), pack2(ep[6], ep[7]));
      *reinterpret_cast<uint2*>(dst + 32) = make_uint2(pack2(en[0], en[1]), pack2(en[2], en[3]));
      *reinterpret_cast<uint2*>(dst + 48) = make_uint2(pack2(en[4], en[5]), pack2(en[6], en[7]));
    }
    {
      bf16x8 bk = *reinterpret_cast<const bf16x8*>(qkv_img + m * QS + 64 + 16 * g);
      f32x4 p0 = __builtin_amdgcn_mfma_f32_16x16x32_bf16(aphi[0], bk, z4, 0, 0, 0);
      f32x4 p1 = __builtin_amdgcn_mfma_f32_16x16x32_bf16(aphi[1], bk, z4, 0, 0, 0);
      float ep[8], en[8];
      softmax_pm(p0, p1, ep, en);
      char* dst = pk_img + m * 128 + 8 * g;
      *reinterpret_cast<uint2*>(dst)      = make_uint2(pack2(ep[0], ep[1]), pack2(ep[2], ep[3]));
      *reinterpret_cast<uint2*>(dst + 32) = make_uint2(pack2(ep[4], ep[5]), pack2(ep[6], ep[7]));
      *reinterpret_cast<uint2*>(dst + 64) = make_uint2(pack2(en[0], en[1]), pack2(en[2], en[3]));
      *reinterpret_cast<uint2*>(dst + 96) = make_uint2(pack2(en[4], en[5]), pack2(en[6], en[7]));
    }
  }
  lds_barrier();
  STAMP(2, 2);
  {
    f32x4 c0 = {0.f, 0.f, 0.f, 0.f}, cz = c0;
    bf16x8 ones;
#pragma unroll
    for (int j = 0; j < 8; ++j) ones[j] = (short)0x3f80;
    const int q = l15 >> 2, p = lane & 3, ft = wid & 3, dt = wid >> 2;
#pragma unroll
    for (int ks = 0; ks < 8; ++ks) {
      const int r0 = ks * 32 + 8 * g + q;
      s16x4 a0 = __builtin_amdgcn_ds_read_tr16_b64_v4i16((LDSP(s16x4))(pk_img + (r0) * 128 + (16 * ft + 4 * p) * 2));
      s16x4 a1 = __builtin_amdgcn_ds_read_tr16_b64_v4i16((LDSP(s16x4))(pk_img + (r0 + 4) * 128 + (16 * ft + 4 * p) * 2));
      bf16x8 afr = __builtin_shufflevector(a0, a1, 0, 1, 2, 3, 4, 5, 6, 7);
      s16x4 b0 = __builtin_amdgcn_ds_read_tr16_b64_v4i16((LDSP(s16x4))(qkv_img + (r0) * QS + 128 + (16 * dt + 4 * p) * 2));
      s16x4 b1 = __builtin_amdgcn_ds_read_tr16_b64_v4i16((LDSP(s16x4))(qkv_img + (r0 + 4) * QS + 128 + (16 * dt + 4 * p) * 2));
      bf16x8 bf0 = __builtin_shufflevector(b0, b1, 0, 1, 2, 3, 4, 5, 6, 7);
      c0 = __builtin_amdgcn_mfma_f32_16x16x32_bf16(afr, bf0, c0, 0, 0, 0);
      cz = __builtin_amdgcn_mfma_f32_16x16x32_bf16(afr, ones, cz, 0, 0, 0);
    }
    float* kd = kvp + ((size_t)(mtile * H + head) * 64) * 32;
#pragma unroll
    for (int r = 0; r < 4; ++r) {
      const int f = 16 * ft + 4 * g + r;
      kd[f * 32 + 16 * dt + l15] = c0[r];
      if (l15 == 0 && dt == 0) zp[(mtile * H + head) * 64 + f] = cz[r];
    }
  }
  STAMP(2, 3);
}

__global__ __launch_bounds__(256) void k_attn_o(const u16* __restrict__ phq, const float* __restrict__ kvp,
                                                const float* __restrict__ zp, u16* __restrict__ o) {
  __shared__ __attribute__((aligned(16))) u16 kvT[32][64 + 8];
  __shared__ float zs[64];
  const int head = blockIdx.x & 31, chunk = blockIdx.x >> 5;
  const int t = threadIdx.x, wid = t >> 6, lane = t & 63, l15 = lane & 15, g = lane >> 4;
  {
    const int f = t >> 2, d0 = (t & 3) * 8;
    float s[8];
#pragma unroll
    for (int j = 0; j < 8; ++j) s[j] = 0.f;
#pragma unroll
    for (int p = 0; p < 8; ++p) {
      const float* src = kvp + ((size_t)(p * H + head) * 64 + f) * 32 + d0;
      float4 a = *reinterpret_cast<const float4*>(src);
      float4 b2 = *reinterpret_cast<const float4*>(src + 4);
      s[0] += a.x; s[1] += a.y; s[2] += a.z; s[3] += a.w;
      s[4] += b2.x; s[5] += b2.y; s[6] += b2.z; s[7] += b2.w;
    }
#pragma unroll
    for (int j = 0; j < 8; ++j) kvT[d0 + j][f] = f2bf(s[j]);
    if (t < 64) {
      float z = 0.f;
#pragma unroll
      for (int p = 0; p < 8; ++p) z += zp[(p * H + head) * 64 + t];
      zs[t] = rbf(z);
    }
  }
  __syncthreads();
  bf16x8 af[2][2];
#pragma unroll
  for (int dt = 0; dt < 2; ++dt)
#pragma unroll
    for (int kk = 0; kk < 2; ++kk)
      af[dt][kk] = *reinterpret_cast<const bf16x8*>(&kvT[16 * dt + l15][32 * kk + 8 * g]);
  float zr[2][8];
#pragma unroll
  for (int kk = 0; kk < 2; ++kk)
#pragma unroll
    for (int j = 0; j < 8; ++j) zr[kk][j] = zs[32 * kk + 8 * g + j];
#pragma unroll
  for (int mt = 0; mt < 4; ++mt) {
    const int m = chunk * 256 + wid * 64 + mt * 16 + l15;
    const u16* src = phq + ((size_t)m * H + head) * 64 + 8 * g;
    bf16x8 b0 = *reinterpret_cast<const bf16x8*>(src);
    bf16x8 b1 = *reinterpret_cast<const bf16x8*>(src + 32);
    float den = 0.f;
#pragma unroll
    for (int j = 0; j < 8; ++j) den += bf2f((u16)b0[j]) * zr[0][j] + bf2f((u16)b1[j]) * zr[1][j];
    den += __shfl_xor(den, 16); den += __shfl_xor(den, 32);
    den = rbf(rbf(den) + 1e-6f);
    const float rden = 1.f / den;
#pragma unroll
    for (int dt = 0; dt < 2; ++dt) {
      f32x4 c = {0.f, 0.f, 0.f, 0.f};
      c = __builtin_amdgcn_mfma_f32_16x16x32_bf16(af[dt][0], b0, c, 0, 0, 0);
      c = __builtin_amdgcn_mfma_f32_16x16x32_bf16(af[dt][1], b1, c, 0, 0, 0);
      *reinterpret_cast<uint2*>(o + (size_t)m * D + head * 32 + 16 * dt + 4 * g) =
          make_uint2(pack2(rbf(c[0]) * rden, rbf(c[1]) * rden), pack2(rbf(c[2]) * rden, rbf(c[3]) * rden));
    }
  }
}

__global__ __launch_bounds__(512) void k_oproj(const u16* __restrict__ o, const u16* __restrict__ wo,
                                               const u16* __restrict__ x, const float* __restrict__ modpart,
                                               u16* __restrict__ x1) {
  extern __shared__ __attribute__((aligned(16))) char lds[];
  const int b = blockIdx.x, xcd = b & 7, idx = b >> 3;
  const int mtile = 2 * xcd + (idx & 1), ntile = idx >> 1;
  const int m0 = mtile * 128, n0 = ntile * 64;
  STAMP(4, 0);
  const int t = threadIdx.x, wid = t >> 6, lane = t & 63, l15 = lane & 15, g = lane >> 4;
  f32x4 acc[2][2];
#pragma unroll
  for (int i = 0; i < 2; ++i)
#pragma unroll
    for (int j = 0; j < 2; ++j) acc[i][j] = (f32x4){0.f, 0.f, 0.f, 0.f};
  gemm_core<128, 4, 4, 2, 6>(o, wo, D, m0, [&](int nb) { return n0 + nb * 16; }, lds, acc);
  const int wm = wid >> 1, wn = wid & 1;
  STAMP(4, 1);
#pragma unroll
  for (int nb = 0; nb < 2; ++nb) {
    const int n = n0 + (wn * 2 + nb) * 16 + 4 * g;
    float ga[4];
#pragma unroll
    for (int v = 0; v < 2; ++v) {}
    {
      float4 a = *reinterpret_cast<const float4*>(modpart + 2 * D + n);
#pragma unroll
      for (int p = 1; p < 4; ++p) {
        float4 a2 = *reinterpret_cast<const float4*>(modpart + p * 6144 + 2 * D + n);
        a.x += a2.x; a.y += a2.y; a.z += a2.z; a.w += a2.w;
      }
      ga[0] = rbf(a.x); ga[1] = rbf(a.y); ga[2] = rbf(a.z); ga[3] = rbf(a.w);
    }
#pragma unroll
    for (int mt = 0; mt < 2; ++mt) {
      const int m = m0 + wm * 32 + mt * 16 + l15;
      uint2 xv = *reinterpret_cast<const uint2*>(x + (size_t)m * D + n);
      float r0 = lo16(xv.x) + rbf(ga[0] * rbf(acc[nb][mt][0]));
      float r1 = hi16(xv.x) + rbf(ga[1] * rbf(acc[nb][mt][1]));
      float r2 = lo16(xv.y) + rbf(ga[2] * rbf(acc[nb][mt][2]));
      float r3 = hi16(xv.y) + rbf(ga[3] * rbf(acc[nb][mt][3]));
      *reinterpret_cast<uint2*>(x1 + (size_t)m * D + n) = make_uint2(pack2(r0, r1), pack2(r2, r3));
    }
  }
  STAMP(4, 2);
}

#define RLX_AGENT __ATOMIC_RELAXED, __HIP_MEMORY_SCOPE_AGENT
DI void st_agent(float* p, float v) { __hip_atomic_store(p, v, RLX_AGENT); }
DI float ld_agent(const float* p) { return __hip_atomic_load(p, RLX_AGENT); }
DI void signal(unsigned* ctr) {
  asm volatile("s_waitcnt vmcnt(0)" ::: "memory");
  __syncthreads();
  if (threadIdx.x == 0) __hip_atomic_fetch_add(ctr, 1u, RLX_AGENT);
}
DI void wait_ctr(unsigned* ctr, unsigned target) {
  if (threadIdx.x == 0) {
    for (unsigned spins = 0; spins < 4000000u; ++spins) {
      if (__hip_atomic_load(ctr, RLX_AGENT) >= target) break;
      __builtin_amdgcn_s_sleep(1);
    }
  }
  __syncthreads();
}

DI void ln_row(const u16* xr, int lane, float (&f)[16], float& mean, float& rstd) {
  uint4 v0 = *reinterpret_cast<const uint4*>(xr + lane * 8);
  uint4 v1 = *reinterpret_cast<const uint4*>(xr + 512 + lane * 8);
  f[0] = lo16(v0.x); f[1] = hi16(v0.x); f[2] = lo16(v0.y); f[3] = hi16(v0.y);
  f[4] = lo16(v0.z); f[5] = hi16(v0.z); f[6] = lo16(v0.w); f[7] = hi16(v0.w);
  f[8] = lo16(v1.x); f[9] = hi16(v1.x); f[10] = lo16(v1.y); f[11] = hi16(v1.y);
  f[12] = lo16(v1.z); f[13] = hi16(v1.z); f[14] = lo16(v1.w); f[15] = hi16(v1.w);
  float s = 0.f;
#pragma unroll
  for (int j = 0; j < 16; ++j) s += f[j];
#pragma unroll
  for (int m = 1; m < 64; m <<= 1) s += __shfl_xor(s, m);
  mean = s * (1.f / D);
  float q = 0.f;
#pragma unroll
  for (int j = 0; j < 16; ++j) { float d = f[j] - mean; q += d * d; }
#pragma unroll
  for (int m = 1; m < 64; m <<= 1) q += __shfl_xor(q, m);
  rstd = rsqrtf(q * (1.f / D) + 1e-5f);
}

__global__ __launch_bounds__(256) void k_ln2(const u16* __restrict__ x1, const float* __restrict__ modpart,
                                             const u16* __restrict__ wr, u16* __restrict__ h2,
                                             u16* __restrict__ out, float* __restrict__ r) {
  __shared__ float sc[D], sh[D], gbv[D];
  const int t = threadIdx.x, wid = t >> 6, lane = t & 63;
  uint4 xv[2][2];
#pragma unroll
  for (int q2 = 0; q2 < 2; ++q2) {
    const u16* xr = x1 + (size_t)(blockIdx.x * 8 + wid * 2 + q2) * D;
    xv[q2][0] = *reinterpret_cast<const uint4*>(xr + lane * 8);
    xv[q2][1] = *reinterpret_cast<const uint4*>(xr + 512 + lane * 8);
  }
  uint4 w0 = *reinterpret_cast<const uint4*>(wr + lane * 8);
  uint4 w1 = *reinterpret_cast<const uint4*>(wr + 512 + lane * 8);
  {
    const int c4 = t * 4;
    float4 a = *reinterpret_cast<const float4*>(modpart + 3 * D + c4);
    float4 bb = *reinterpret_cast<const float4*>(modpart + 4 * D + c4);
    float4 c = *reinterpret_cast<const float4*>(modpart + 5 * D + c4);
#pragma unroll
    for (int p = 1; p < 4; ++p) {
      float4 a2 = *reinterpret_cast<const float4*>(modpart + p * 6144 + 3 * D + c4);
      float4 b2 = *reinterpret_cast<const float4*>(modpart + p * 6144 + 4 * D + c4);
      float4 c2 = *reinterpret_cast<const float4*>(modpart + p * 6144 + 5 * D + c4);
      a.x += a2.x; a.y += a2.y; a.z += a2.z; a.w += a2.w;
      bb.x += b2.x; bb.y += b2.y; bb.z += b2.z; bb.w += b2.w;
      c.x += c2.x; c.y += c2.y; c.z += c2.z; c.w += c2.w;
    }
    sh[c4] = rbf(a.x); sh[c4 + 1] = rbf(a.y); sh[c4 + 2] = rbf(a.z); sh[c4 + 3] = rbf(a.w);
    sc[c4] = rbf(1.f + rbf(bb.x)); sc[c4 + 1] = rbf(1.f + rbf(bb.y));
    sc[c4 + 2] = rbf(1.f + rbf(bb.z)); sc[c4 + 3] = rbf(1.f + rbf(bb.w));
    gbv[c4] = rbf(c.x); gbv[c4 + 1] = rbf(c.y); gbv[c4 + 2] = rbf(c.z); gbv[c4 + 3] = rbf(c.w);
  }
  __syncthreads();
  float wf[16];
  wf[0] = lo16(w0.x); wf[1] = hi16(w0.x); wf[2] = lo16(w0.y); wf[3] = hi16(w0.y);
  wf[4] = lo16(w0.z); wf[5] = hi16(w0.z); wf[6] = lo16(w0.w); wf[7] = hi16(w0.w);
  wf[8] = lo16(w1.x); wf[9] = hi16(w1.x); wf[10] = lo16(w1.y); wf[11] = hi16(w1.y);
  wf[12] = lo16(w1.z); wf[13] = hi16(w1.z); wf[14] = lo16(w1.w); wf[15] = hi16(w1.w);
#pragma unroll
  for (int q2 = 0; q2 < 2; ++q2) {
    const int row = blockIdx.x * 8 + wid * 2 + q2;
    const uint4 v0 = xv[q2][0], v1 = xv[q2][1];
    float f[16];
    f[0] = lo16(v0.x); f[1] = hi16(v0.x); f[2] = lo16(v0.y); f[3] = hi16(v0.y);
    f[4] = lo16(v0.z); f[5] = hi16(v0.z); f[6] = lo16(v0.w); f[7] = hi16(v0.w);
    f[8] = lo16(v1.x); f[9] = hi16(v1.x); f[10] = lo16(v1.y); f[11] = hi16(v1.y);
    f[12] = lo16(v1.z); f[13] = hi16(v1.z); f[14] = lo16(v1.w); f[15] = hi16(v1.w);
    float s = 0.f;
#pragma unroll
    for (int j = 0; j < 16; ++j) s += f[j];
#pragma unroll
    for (int m = 1; m < 64; m <<= 1) s += __shfl_xor(s, m);
    const float mean = s * (1.f / D);
    float q = 0.f;
#pragma unroll
    for (int j = 0; j < 16; ++j) { float d = f[j] - mean; q += d * d; }
#pragma unroll
    for (int m = 1; m < 64; m <<= 1) q += __shfl_xor(q, m);
    const float rstd = rsqrtf(q * (1.f / D) + 1e-5f);
    unsigned ho[8], oo[8];
    float dot = 0.f;
#pragma unroll
    for (int j = 0; j < 16; j += 2) {
      const int c0 = (j < 8) ? lane * 8 + j : 512 + lane * 8 + (j - 8);
      float a = rbf(rbf((f[j] - mean) * rstd) * sc[c0] + sh[c0]);
      float bq = rbf(rbf((f[j + 1] - mean) * rstd) * sc[c0 + 1] + sh[c0 + 1]);
      dot += a * wf[j] + bq * wf[j + 1];
      ho[j >> 1] = pack2(a, bq);
      oo[j >> 1] = pack2(f[j] + rbf(gbv[c0] * a), f[j + 1] + rbf(gbv[c0 + 1] * bq));
    }
#pragma unroll
    for (int m = 1; m < 64; m <<= 1) dot += __shfl_xor(dot, m);
    u16* hr = h2 + (size_t)row * D;
    *reinterpret_cast<uint4*>(hr + lane * 8) = make_uint4(ho[0], ho[1], ho[2], ho[3]);
    *reinterpret_cast<uint4*>(hr + 512 + lane * 8) = make_uint4(ho[4], ho[5], ho[6], ho[7]);
    u16* orow = out + (size_t)row * D;
    *reinterpret_cast<uint4*>(orow + lane * 8) = make_uint4(oo[0], oo[1], oo[2], oo[3]);
    *reinterpret_cast<uint4*>(orow + 512 + lane * 8) = make_uint4(oo[4], oo[5], oo[6], oo[7]);
    if (lane == 0) r[row] = rbf(dot);
  }
}

__global__ __launch_bounds__(256) void k_moe(const float* __restrict__ r, const u16* __restrict__ h2,
                                             const u16* __restrict__ x1, const float* __restrict__ modpart,
                                             const u16* __restrict__ wg, const u16* __restrict__ wu,
                                             const u16* __restrict__ wd, u16* __restrict__ out,
                                             float* __restrict__ gup, float* __restrict__ dpart,
                                             unsigned* __restrict__ ctr) {
  __shared__ float red[32][65];
  __shared__ float hs[2][256];
  __shared__ float sm_v[8]; __shared__ int sm_i[8];
  __shared__ int s_last;
  const int b = blockIdx.x, t = threadIdx.x, wid = t >> 6, lane = t & 63;
  const int cq = t & 7, rr = t >> 3;
  const int gcc = b % 56, gks = b / 56;
  const int dcc = b & 15, dks = b >> 4;
  float rv8[8];
#pragma unroll
  for (int i = 0; i < 8; ++i) rv8[i] = r[t + i * 256];
  uint4 wa[8], wb[8];
#pragma unroll
  for (int i = 0; i < 8; ++i) {
    const int k = gks * 256 + i * 32 + rr;
    wa[i] = *reinterpret_cast<const uint4*>(wg + (size_t)k * F + gcc * 64 + cq * 8);
    wb[i] = *reinterpret_cast<const uint4*>(wu + (size_t)k * F + gcc * 64 + cq * 8);
  }
  int i0, i1; float r0, r1;
  {
    float b0 = -INFINITY, b1 = -INFINITY; int j0 = 0x7fffffff, j1 = 0x7fffffff;
#pragma unroll
    for (int i = 0; i < 8; ++i) {
      const int idx = t + i * 256;
      const float v = rv8[i];
      if (v > b0 || (v == b0 && idx < j0)) { b1 = b0; j1 = j0; b0 = v; j0 = idx; }
      else if (v > b1 || (v == b1 && idx < j1)) { b1 = v; j1 = idx; }
    }
#pragma unroll
    for (int m = 1; m < 64; m <<= 1) {
      float o0 = __shfl_xor(b0, m), o1 = __shfl_xor(b1, m);
      int p0 = __shfl_xor(j0, m), p1 = __shfl_xor(j1, m);
      if (o0 > b0 || (o0 == b0 && p0 < j0)) {
        if (b0 > o1 || (b0 == o1 && j0 < p1)) { b1 = b0; j1 = j0; } else { b1 = o1; j1 = p1; }
        b0 = o0; j0 = p0;
      } else {
        if (o0 > b1 || (o0 == b1 && p0 < j1)) { b1 = o0; j1 = p0; }
      }
    }
    if (lane == 0) { sm_v[wid * 2] = b0; sm_v[wid * 2 + 1] = b1; sm_i[wid * 2] = j0; sm_i[wid * 2 + 1] = j1; }
    __syncthreads();
    b0 = sm_v[0]; b1 = sm_v[1]; j0 = sm_i[0]; j1 = sm_i[1];
#pragma unroll
    for (int w = 1; w < 4; ++w) {
      float o0 = sm_v[w * 2], o1 = sm_v[w * 2 + 1];
      int p0 = sm_i[w * 2], p1 = sm_i[w * 2 + 1];
      if (o0 > b0 || (o0 == b0 && p0 < j0)) {
        if (b0 > o1 || (b0 == o1 && j0 < p1)) { b1 = b0; j1 = j0; } else { b1 = o1; j1 = p1; }
        b0 = o0; j0 = p0;
      } else {
        if (o0 > b1 || (o0 == b1 && p0 < j1)) { b1 = o0; j1 = p0; }
      }
    }
    i0 = j0; i1 = j1; r0 = b0; r1 = b1;
  }
  float hv0[8], hv1[8];
#pragma unroll
  for (int i = 0; i < 8; ++i) {
    const int k = gks * 256 + i * 32 + rr;
    hv0[i] = bf2f(h2[(size_t)i0 * D + k]); hv1[i] = bf2f(h2[(size_t)i1 * D + k]);
  }
  uint4 wdv[8];
#pragma unroll
  for (int i = 0; i < 8; ++i) {
    const int k = dks * 256 + i * 32 + rr;
    wdv[i] = *reinterpret_cast<const uint4*>(wd + (size_t)k * D + dcc * 64 + cq * 8);
  }
  {
    float acc[4][8];
#pragma unroll
    for (int a = 0; a < 4; ++a)
#pragma unroll
      for (int j = 0; j < 8; ++j) acc[a][j] = 0.f;
#pragma unroll
    for (int i = 0; i < 8; ++i) {
      const float h0 = hv0[i], h1v = hv1[i];
      const unsigned wv[4] = {wa[i].x, wa[i].y, wa[i].z, wa[i].w};
      const unsigned uv[4] = {wb[i].x, wb[i].y, wb[i].z, wb[i].w};
#pragma unroll
      for (int j = 0; j < 4; ++j) {
        acc[0][2 * j] += h0 * lo16(wv[j]);  acc[0][2 * j + 1] += h0 * hi16(wv[j]);
        acc[1][2 * j] += h1v * lo16(wv[j]); acc[1][2 * j + 1] += h1v * hi16(wv[j]);
        acc[2][2 * j] += h0 * lo16(uv[j]);  acc[2][2 * j + 1] += h0 * hi16(uv[j]);
        acc[3][2 * j] += h1v * lo16(uv[j]); acc[3][2 * j + 1] += h1v * hi16(uv[j]);
      }
    }
#pragma unroll
    for (int a = 0; a < 4; ++a) {
      __syncthreads();
#pragma unroll
      for (int j = 0; j < 8; ++j) red[rr][cq * 8 + j] = acc[a][j];
      __syncthreads();
      if (t < 64) {
        float sum = 0.f;
#pragma unroll
        for (int i = 0; i < 32; ++i) sum += red[i][t];
        st_agent(gup + (size_t)(gks * 4 + a) * F + gcc * 64 + t, sum);
      }
    }
  }
  signal(ctr + 16 + (gcc >> 2));
  wait_ctr(ctr + 16 + dks, 16);
  {
    const int k = dks * 256 + t;
#pragma unroll
    for (int tok = 0; tok < 2; ++tok) {
      float gsum = 0.f, usum = 0.f;
#pragma unroll
      for (int p = 0; p < 4; ++p) {
        gsum += ld_agent(gup + (size_t)(p * 4 + tok) * F + k);
        usum += ld_agent(gup + (size_t)(p * 4 + 2 + tok) * F + k);
      }
      gsum = rbf(gsum); usum = rbf(usum);
      const float sg = rbf(gsum / (1.f + __expf(-gsum)));
      hs[tok][t] = rbf(sg * usum);
    }
  }
  __syncthreads();
  {
    float ac2[2][8];
#pragma unroll
    for (int a = 0; a < 2; ++a)
#pragma unroll
      for (int j = 0; j < 8; ++j) ac2[a][j] = 0.f;
#pragma unroll
    for (int i = 0; i < 8; ++i) {
      const int kl = i * 32 + rr;
      const float h0 = hs[0][kl], h1v = hs[1][kl];
      const unsigned wv[4] = {wdv[i].x, wdv[i].y, wdv[i].z, wdv[i].w};
#pragma unroll
      for (int j = 0; j < 4; ++j) {
        ac2[0][2 * j] += h0 * lo16(wv[j]);  ac2[0][2 * j + 1] += h0 * hi16(wv[j]);
        ac2[1][2 * j] += h1v * lo16(wv[j]); ac2[1][2 * j + 1] += h1v * hi16(wv[j]);
      }
    }
#pragma unroll
    for (int a = 0; a < 2; ++a) {
      __syncthreads();
#pragma unroll
      for (int j = 0; j < 8; ++j) red[rr][cq * 8 + j] = ac2[a][j];
      __syncthreads();
      if (t < 64) {
        float sum = 0.f;
#pragma unroll
        for (int i = 0; i < 32; ++i) sum += red[i][t];
        st_agent(dpart + (size_t)(dks * 2 + a) * D + dcc * 64 + t, sum);
      }
    }
  }
  asm volatile("s_waitcnt vmcnt(0)" ::: "memory");
  __syncthreads();
  if (t == 0) s_last = (__hip_atomic_fetch_add(ctr + 32 + dcc, 1u, RLX_AGENT) == 13u);
  __syncthreads();
  if (s_last && t < 128) {
    const int tok = t >> 6, n = dcc * 64 + (t & 63);
    const int row = tok ? i1 : i0;
    const float rv = tok ? r1 : r0;
    float y = 0.f;
#pragma unroll
    for (int p = 0; p < 14; ++p) y += ld_agent(dpart + (size_t)(p * 2 + tok) * D + n);
    const float sig = rbf(1.f / (1.f + __expf(-rv)));
    y = rbf(rbf(y) * sig);
    float gb = 0.f;
#pragma unroll
    for (int p = 0; p < 4; ++p) gb += modpart[p * 6144 + 5 * D + n];
    gb = rbf(gb);
    const float hv = bf2f(h2[(size_t)row * D + n]);
    const float xv = bf2f(x1[(size_t)row * D + n]);
    out[(size_t)row * D + n] = f2bf(xv + rbf(gb * rbf(hv + y)));
  }
}

extern "C" void kernel_launch(void* const* d_in, const int* in_sizes, int n_in, void* d_out, int out_size,
                              void* d_ws, size_t ws_size, hipStream_t stream) {
  const u16* x    = (const u16*)d_in[0];
  const u16* wqkv = (const u16*)d_in[1];
  const u16* wphi = (const u16*)d_in[2];
  const u16* wo   = (const u16*)d_in[3];
  const u16* w_r  = (const u16*)d_in[5];
  const u16* wg   = (const u16*)d_in[6];
  const u16* wu   = (const u16*)d_in[7];
  const u16* wd   = (const u16*)d_in[8];
  const u16* c    = (const u16*)d_in[9];
  const u16* wmod = (const u16*)d_in[10];
  u16* out = (u16*)d_out;

  char* ws = (char*)d_ws;
  size_t off = 0;
  auto alloc = [&](size_t bytes) { char* p = ws + off; off += (bytes + 255) & ~(size_t)255; return p; };
  float* modpart = (float*)alloc(4 * 6144 * 4);
  u16* h1  = (u16*)alloc((size_t)S * D * 2);
  u16* phq = (u16*)alloc((size_t)S * H * 64 * 2);
  float* kvp = (float*)alloc((size_t)8 * H * 64 * 32 * 4);
  float* zp  = (float*)alloc((size_t)8 * H * 64 * 4);
  u16* o   = (u16*)alloc((size_t)S * D * 2);
  u16* x1  = (u16*)alloc((size_t)S * D * 2);
  u16* h2  = (u16*)alloc((size_t)S * D * 2);
  float* r = (float*)alloc(S * 4);
  float* gup = (float*)alloc((size_t)16 * F * 4);
  float* dpart = (float*)alloc((size_t)28 * D * 4);
  unsigned* ctr = (unsigned*)alloc(256);

  k_mod<<<384, 256, 0, stream>>>(c, wmod, modpart, ctr);
  k_ln1<<<256, 256, 0, stream>>>(x, modpart, h1);
  constexpr int LDS_QKV = 3 * (256 * 128 + 64 * 192);
  constexpr int LDS_OP  = 6 * (128 * 128 + 64 * 128);
  hipFuncSetAttribute((const void*)k_qkv, hipFuncAttributeMaxDynamicSharedMemorySize, LDS_QKV);
  hipFuncSetAttribute((const void*)k_oproj, hipFuncAttributeMaxDynamicSharedMemorySize, LDS_OP);
  k_qkv<<<256, 512, LDS_QKV, stream>>>(h1, wqkv, wphi, phq, kvp, zp);
  k_attn_o<<<256, 256, 0, stream>>>(phq, kvp, zp, o);
  k_oproj<<<256, 512, LDS_OP, stream>>>(o, wo, x, modpart, x1);
  k_ln2<<<256, 256, 0, stream>>>(x1, modpart, w_r, h2, out, r);
  k_moe<<<224, 256, 0, stream>>>(r, h2, x1, modpart, wg, wu, wd, out, gup, dpart, ctr);
}
```

```cpp
#include <hip/hip_runtime.h>
#include <hip/hip_bf16.h>
#include <stdint.h>
#define PFB 0
#define OPST 3

constexpr int S  = 2048;
constexpr int D  = 1024;
constexpr int H  = 32;
constexpr int F  = 3584;

typedef short  s16x4  __attribute__((ext_vector_type(4)));
typedef short  bf16x8 __attribute__((ext_vector_type(8)));
typedef float  f32x4  __attribute__((ext_vector_type(4)));
typedef unsigned short u16;
#define LDSP(T) __attribute__((address_space(3))) T*
#define DI __device__ __forceinline__
#define STAMP(k, i) do {} while (0)

DI float bf2f(u16 x) { return __uint_as_float(((unsigned)x) << 16); }
typedef __bf16 bf16v2 __attribute__((ext_vector_type(2)));
typedef float f32v2 __attribute__((ext_vector_type(2)));
DI u16 f2bf(float f) { __bf16 b = (__bf16)f; return __builtin_bit_cast(u16, b); }
DI float rbf(float f) { return (float)(__bf16)f; }
DI unsigned pack2(float a, float b) {
  f32v2 v = {a, b};
  return __builtin_bit_cast(unsigned, __builtin_convertvector(v, bf16v2));
}
DI float lo16(unsigned u) { return __uint_as_float(u << 16); }
DI float hi16(unsigned u) { return __uint_as_float(u & 0xffff0000u); }

__global__ __launch_bounds__(256) void k_mod(const u16* __restrict__ c, const u16* __restrict__ wmod,
                                             float* __restrict__ part, unsigned* __restrict__ ctr) {
  if (blockIdx.x == 0 && threadIdx.x < 64) ctr[threadIdx.x] = 0u;
  __shared__ float red[32][65];
  const int cc = blockIdx.x % 96, ks = blockIdx.x / 96;
  const int t = threadIdx.x, cq = t & 7, rr = t >> 3;
  const int col0 = cc * 64 + cq * 8;
  uint4 w[8];
#pragma unroll
  for (int i = 0; i < 8; ++i) {
    const int k = ks * 256 + i * 32 + rr;
    w[i] = *reinterpret_cast<const uint4*>(wmod + (size_t)k * 6144 + col0);
  }
  float acc[8];
#pragma unroll
  for (int j = 0; j < 8; ++j) acc[j] = 0.f;
#pragma unroll
  for (int i = 0; i < 8; ++i) {
    const int k = ks * 256 + i * 32 + rr;
    const float cv = bf2f(c[k]);
    const float s = rbf(cv / (1.f + __expf(-cv)));
    acc[0] += s * lo16(w[i].x); acc[1] += s * hi16(w[i].x);
    acc[2] += s * lo16(w[i].y); acc[3] += s * hi16(w[i].y);
    acc[4] += s * lo16(w[i].z); acc[5] += s * hi16(w[i].z);
    acc[6] += s * lo16(w[i].w); acc[7] += s * hi16(w[i].w);
  }
#pragma unroll
  for (int j = 0; j < 8; ++j) red[rr][cq * 8 + j] = acc[j];
  __syncthreads();
  if (t < 64) {
    float s = 0.f;
#pragma unroll
    for (int i = 0; i < 32; ++i) s += red[i][t];
    part[ks * 6144 + cc * 64 + t] = s;
  }
}

__global__ __launch_bounds__(256) void k_ln1(const u16* __restrict__ x, const float* __restrict__ modpart,
                                             u16* __restrict__ h) {
  __shared__ float sc[D], sh[D];
  const int t = threadIdx.x;
  {
    const int c4 = t * 4;
    float4 a = *reinterpret_cast<const float4*>(modpart + c4);
    float4 b = *reinterpret_cast<const float4*>(modpart + D + c4);
#pragma unroll
    for (int p = 1; p < 4; ++p) {
      float4 a2 = *reinterpret_cast<const float4*>(modpart + p * 6144 + c4);
      float4 b2 = *reinterpret_cast<const float4*>(modpart + p * 6144 + D + c4);
      a.x += a2.x; a.y += a2.y; a.z += a2.z; a.w += a2.w;
      b.x += b2.x; b.y += b2.y; b.z += b2.z; b.w += b2.w;
    }
    sh[c4] = rbf(a.x); sh[c4 + 1] = rbf(a.y); sh[c4 + 2] = rbf(a.z); sh[c4 + 3] = rbf(a.w);
    sc[c4] = rbf(1.f + rbf(b.x)); sc[c4 + 1] = rbf(1.f + rbf(b.y));
    sc[c4 + 2] = rbf(1.f + rbf(b.z)); sc[c4 + 3] = rbf(1.f + rbf(b.w));
  }
  __syncthreads();
  const int wid = t >> 6, lane = t & 63;
#pragma unroll
  for (int rr = 0; rr < 2; ++rr) {
    const int row = blockIdx.x * 8 + wid * 2 + rr;
    const u16* xr = x + (size_t)row * D;
    uint4 v0 = *reinterpret_cast<const uint4*>(xr + lane * 8);
    uint4 v1 = *reinterpret_cast<const uint4*>(xr + 512 + lane * 8);
    float f[16];
    f[0] = lo16(v0.x); f[1] = hi16(v0.x); f[2] = lo16(v0.y); f[3] = hi16(v0.y);
    f[4] = lo16(v0.z); f[5] = hi16(v0.z); f[6] = lo16(v0.w); f[7] = hi16(v0.w);
    f[8] = lo16(v1.x); f[9] = hi16(v1.x); f[10] = lo16(v1.y); f[11] = hi16(v1.y);
    f[12] = lo16(v1.z); f[13] = hi16(v1.z); f[14] = lo16(v1.w); f[15] = hi16(v1.w);
    float s = 0.f;
#pragma unroll
    for (int j = 0; j < 16; ++j) s += f[j];
#pragma unroll
    for (int m = 1; m < 64; m <<= 1) s += __shfl_xor(s, m);
    const float mean = s * (1.f / D);
    float q = 0.f;
#pragma unroll
    for (int j = 0; j < 16; ++j) { float d = f[j] - mean; q += d * d; }
#pragma unroll
    for (int m = 1; m < 64; m <<= 1) q += __shfl_xor(q, m);
    const float rstd = rsqrtf(q * (1.f / D) + 1e-5f);
    unsigned o[8];
#pragma unroll
    for (int j = 0; j < 16; j += 2) {
      const int c0 = (j < 8) ? lane * 8 + j : 512 + lane * 8 + (j - 8);
      float a = rbf((f[j] - mean) * rstd) * sc[c0] + sh[c0];
      float b = rbf((f[j + 1] - mean) * rstd) * sc[c0 + 1] + sh[c0 + 1];
      o[j >> 1] = pack2(a, b);
    }
    u16* hr = h + (size_t)row * D;
    *reinterpret_cast<uint4*>(hr + lane * 8) = make_uint4(o[0], o[1], o[2], o[3]);
    *reinterpret_cast<uint4*>(hr + 512 + lane * 8) = make_uint4(o[4], o[5], o[6], o[7]);
  }
}

template <int UK> DI int xswz(int row, int c) {
  if (UK == 64) return c ^ ((row >> 1) & 7);
  else return c ^ ((0x78 >> (2 * ((row >> 2) & 3))) & 3);
}
template <int PPR> DI int wperm(int k, int j) {
  if (PPR == 12) { int v = j + 2 * ((k >> 3) & 1); return v >= 12 ? v - 12 : v; }
  else return j ^ (2 * (((k >> 1) & 1) | (((k >> 3) & 1) << 1)));
}
template <int PPR> DI int wperm_inv(int k, int pos) {
  if (PPR == 12) { int v = pos - 2 * ((k >> 3) & 1); return v < 0 ? v + 12 : v; }
  else return pos ^ (2 * (((k >> 1) & 1) | (((k >> 3) & 1) << 1)));
}

template <int BM, int NB, int WM, int WN, int NST, typename ColMap>
DI void gemm_core(const u16* __restrict__ X, const u16* __restrict__ W, int ldw, int m0, ColMap colmap, char* lds,
                  f32x4 (&acc)[NB / WN][BM / WM / 16]) {
  constexpr int UK = 64;
  constexpr int MT = BM / WM / 16, NW = NB / WN;
  constexpr int XRB = UK * 2, CPR = UK / 8;
  constexpr int PPR = NB * 2, WRB = NB * 32;
  constexpr int XB = BM * XRB, WB = UK * WRB, UB = XB + WB;
  constexpr int XT = XB / 1024, WT = WB / 1024;
  constexpr int XI = XT / 8, WI = (WT + 7) / 8, WMIN = WT / 8;
  constexpr int NU = D / UK;
  static_assert(XT % 8 == 0, "");
  const int t = threadIdx.x, wid = __builtin_amdgcn_readfirstlane(t >> 6), lane = t & 63;
  const u16* xsrc[XI];
#pragma unroll
  for (int i = 0; i < XI; ++i) {
    const int p = (wid * XI + i) * 64 + lane;
    const int row = p / CPR, c = xswz<UK>(row, p % CPR);
    xsrc[i] = X + (size_t)(m0 + row) * D + c * 8;
  }
  const u16* wsrc[WI];
#pragma unroll
  for (int i = 0; i < WI; ++i) {
    const int p = (wid + i * 8) * 64 + lane;
    const int k = (p / PPR) % UK, j = wperm_inv<PPR>(k, p % PPR);
    wsrc[i] = W + (size_t)k * ldw + colmap(j >> 1) + 8 * (j & 1);
  }
  auto issue = [&](int u) {
    char* base = lds + (u % NST) * UB;
#pragma unroll
    for (int i = 0; i < XI; ++i)
      __builtin_amdgcn_global_load_lds((const unsigned*)(xsrc[i] + u * UK), (LDSP(unsigned))(base + (wid * XI + i) * 1024), 16, 0, 0);
#pragma unroll
    for (int i = 0; i < WI; ++i)
      if (wid + i * 8 < WT)
        __builtin_amdgcn_global_load_lds((const unsigned*)(wsrc[i] + (size_t)u * UK * ldw), (LDSP(unsigned))(base + XB + (wid + i * 8) * 1024), 16, 0, 0);
  };
  const int wm = wid / WN, wn = wid % WN;
  const int l15 = lane & 15, g = lane >> 4, q = l15 >> 2, p = lane & 3;
  int xoff[MT][2], woff[NW][2][2];
#pragma unroll
  for (int kk = 0; kk < 2; ++kk) {
#pragma unroll
    for (int mt = 0; mt < MT; ++mt) {
      const int row = wm * (BM / WM) + mt * 16 + l15;
      xoff[mt][kk] = row * XRB + xswz<UK>(row, kk * 4 + g) * 16;
    }
#pragma unroll
    for (int nb = 0; nb < NW; ++nb)
#pragma unroll
      for (int tt = 0; tt < 2; ++tt) {
        const int k = kk * 32 + 8 * g + 4 * tt + q;
        woff[nb][kk][tt] = XB + k * WRB + wperm<PPR>(k, 2 * (wn * NW + nb) + (p >> 1)) * 16 + 8 * (p & 1);
      }
  }
  bf16x8 fa[2][NW], fb[2][MT];
  auto rd = [&](int kk, int u) {
    const char* base = lds + (u % NST) * UB;
#pragma unroll
    for (int mt = 0; mt < MT; ++mt) fb[kk][mt] = *reinterpret_cast<const bf16x8*>(base + xoff[mt][kk]);
#pragma unroll
    for (int nb = 0; nb < NW; ++nb) {
      s16x4 a0 = __builtin_amdgcn_ds_read_tr16_b64_v4i16((LDSP(s16x4))(base + woff[nb][kk][0]));
      s16x4 a1 = __builtin_amdgcn_ds_read_tr16_b64_v4i16((LDSP(s16x4))(base + woff[nb][kk][1]));
      fa[kk][nb] = __builtin_shufflevector(a0, a1, 0, 1, 2, 3, 4, 5, 6, 7);
    }
  };
  auto mm = [&](int kk) {
#pragma unroll
    for (int nb = 0; nb < NW; ++nb)
#pragma unroll
      for (int mt = 0; mt < MT; ++mt)
        acc[nb][mt] = __builtin_amdgcn_mfma_f32_16x16x32_bf16(fa[kk][nb], fb[kk][mt], acc[nb][mt], 0, 0, 0);
  };
#pragma unroll
  for (int u = 0; u < NST - 1; ++u) issue(u);
  asm volatile("s_waitcnt vmcnt(%0)" ::"i"((NST - 2) * (XI + WMIN)) : "memory");
  __builtin_amdgcn_s_barrier();
  rd(0, 0);
  for (int u = 0; u < NU; ++u) {
    if (u + NST - 1 < NU) issue(u + NST - 1);
    rd(1, u);
    mm(0);
    if (u + NST - 1 < NU) { asm volatile("s_waitcnt vmcnt(%0) lgkmcnt(0)" ::"i"((NST - 2) * (XI + WMIN)) : "memory"); }
    else                  { asm volatile("s_waitcnt vmcnt(0) lgkmcnt(0)" ::: "memory"); }
    __builtin_amdgcn_s_barrier();
    if (u + 1 < NU) rd(0, u + 1);
    mm(1);
  }
  __syncthreads();
}

DI void prefetch_range(const void* base, size_t bytes, int nblk, int blk) {
  const size_t per = (bytes / 16 + nblk - 1) / nblk;
  const uint4* p = (const uint4*)base + per * blk;
  const size_t tot = bytes / 16;
  unsigned acc = 0;
  for (size_t i = threadIdx.x; i < per; i += 8 * blockDim.x) {
    uint4 v[8];
#pragma unroll
    for (int j = 0; j < 8; ++j) { size_t idx = i + j * blockDim.x; v[j] = (idx < per && per * blk + idx < tot) ? p[idx] : make_uint4(0, 0, 0, 0); }
#pragma unroll
    for (int j = 0; j < 8; ++j) acc ^= v[j].x ^ v[j].y ^ v[j].z ^ v[j].w;
  }
  asm volatile("" ::"v"(acc));
}

DI void lds_barrier() { asm volatile("s_waitcnt lgkmcnt(0)" ::: "memory"); __builtin_amdgcn_s_barrier(); }

DI float red_x16(float x, bool mx) {
  auto r = __builtin_amdgcn_permlane16_swap(__float_as_uint(x), __float_as_uint(x), false, false);
  float a = __uint_as_float(r[0]), b = __uint_as_float(r[1]);
  return mx ? fmaxf(a, b) : a + b;
}
DI float red_x32(float x, bool mx) {
  auto r = __builtin_amdgcn_permlane32_swap(__float_as_uint(x), __float_as_uint(x), false, false);
  float a = __uint_as_float(r[0]), b = __uint_as_float(r[1]);
  return mx ? fmaxf(a, b) : a + b;
}

DI void softmax_pm(const f32x4& p0, const f32x4& p1, float (&ep)[8], float (&en)[8]) {
  float v[8] = {rbf(p0[0]), rbf(p0[1]), rbf(p0[2]), rbf(p0[3]), rbf(p1[0]), rbf(p1[1]), rbf(p1[2]), rbf(p1[3])};
  float mx = v[0], mn = v[0];
#pragma unroll
  for (int j = 1; j < 8; ++j) { mx = fmaxf(mx, v[j]); mn = fminf(mn, v[j]); }
  float nmn = -mn;
  mx = red_x32(red_x16(mx, true), true);
  nmn = red_x32(red_x16(nmn, true), true);
  float sp = 0.f, sn = 0.f;
#pragma unroll
  for (int j = 0; j < 8; ++j) {
    ep[j] = __expf(v[j] - mx); en[j] = __expf(-nmn - v[j]);
    sp += ep[j]; sn += en[j];
  }
  sp = red_x32(red_x16(sp, false), false);
  sn = red_x32(red_x16(sn, false), false);
  const float rp = __builtin_amdgcn_rcpf(sp), rn = __builtin_amdgcn_rcpf(sn);
#pragma unroll
  for (int j = 0; j < 8; ++j) { ep[j] *= rp; en[j] *= rn; }
}

__global__ __launch_bounds__(512) void k_qkv(const u16* __restrict__ h1, const u16* __restrict__ wqkv,
                                             const u16* __restrict__ wphi, u16* __restrict__ phq,
                                             float* __restrict__ kvp, float* __restrict__ zp) {
  extern __shared__ __attribute__((aligned(16))) char lds[];
  const int b = blockIdx.x, xcd = b & 7, idx = b >> 3;
  const int mtile = 2 * (xcd & 3) + (idx & 1), head = 16 * (xcd >> 2) + (idx >> 1);
  const int m0 = mtile * 256;
  STAMP(2, 0);
  const int t = threadIdx.x, wid = t >> 6, lane = t & 63, l15 = lane & 15, g = lane >> 4;
  bf16x8 aphi[2];
#pragma unroll
  for (int ft = 0; ft < 2; ++ft)
#pragma unroll
    for (int j = 0; j < 8; ++j) aphi[ft][j] = (short)wphi[(head * 32 + 8 * g + j) * 32 + 16 * ft + l15];
  f32x4 acc[3][4];
#pragma unroll
  for (int i = 0; i < 3; ++i)
#pragma unroll
    for (int j = 0; j < 4; ++j) acc[i][j] = (f32x4){0.f, 0.f, 0.f, 0.f};
  gemm_core<256, 6, 4, 2, 3>(h1, wqkv, 3 * D, m0, [&](int nb) { return (nb >> 1) * D + head * 32 + (nb & 1) * 16; }, lds, acc);
  STAMP(2, 1);
  constexpr int QS = 208;
  char* qkv_img = lds;
  char* pk_img = lds + 256 * QS;
  {
    const int wm = wid >> 1, wn = wid & 1;
#pragma unroll
    for (int nb = 0; nb < 3; ++nb)
#pragma unroll
      for (int mt = 0; mt < 4; ++mt) {
        const int m = wm * 64 + mt * 16 + l15, n = (wn * 3 + nb) * 16 + 4 * g;
        *reinterpret_cast<uint2*>(qkv_img + m * QS + n * 2) =
            make_uint2(pack2(acc[nb][mt][0], acc[nb][mt][1]), pack2(acc[nb][mt][2], acc[nb][mt][3]));
      }
  }
  lds_barrier();
#pragma unroll
  for (int mt = 0; mt < 2; ++mt) {
    const int m = wid * 32 + mt * 16 + l15;
    const f32x4 z4 = {0.f, 0.f, 0.f, 0.f};
    {
      bf16x8 bq = *reinterpret_cast<const bf16x8*>(qkv_img + m * QS + 16 * g);
      f32x4 p0 = __builtin_amdgcn_mfma_f32_16x16x32_bf16(aphi[0], bq, z4, 0, 0, 0);
      f32x4 p1 = __builtin_amdgcn_mfma_f32_16x16x32_bf16(aphi[1], bq, z4, 0, 0, 0);
      float ep[8], en[8];
      softmax_pm(p0, p1, ep, en);
      u16* dst = phq + ((size_t)(m0 + m) * H + head) * 64 + 4 * g;
      *reinterpret_cast<uint2*>(dst)      = make_uint2(pack2(ep[0], ep[1]), pack2(ep[2], ep[3]));
      *reinterpret_cast<uint2*>(dst + 16) = make_uint2(pack2(ep[4], ep[5]), pack2(ep[6], ep[7]));
      *reinterpret_cast<uint2*>(dst + 32) = make_uint2(pack2(en[0], en[1]), pack2(en[2], en[3]));
      *reinterpret_cast<uint2*>(dst + 48) = make_uint2(pack2(en[4], en[5]), pack2(en[6], en[7]));
    }
    {
      bf16x8 bk = *reinterpret_cast<const bf16x8*>(qkv_img + m * QS + 64 + 16 * g);
      f32x4 p0 = __builtin_amdgcn_mfma_f32_16x16x32_bf16(aphi[0], bk, z4, 0, 0, 0);
      f32x4 p1 = __builtin_amdgcn_mfma_f32_16x16x32_bf16(aphi[1], bk, z4, 0, 0, 0);
      float ep[8], en[8];
      softmax_pm(p0, p1, ep, en);
      char* dst = pk_img + m * 128 + 8 * g;
      *reinterpret_cast<uint2*>(dst)      = make_uint2(pack2(ep[0], ep[1]), pack2(ep[2], ep[3]));
      *reinterpret_cast<uint2*>(dst + 32) = make_uint2(pack2(ep[4], ep[5]), pack2(ep[6], ep[7]));
      *reinterpret_cast<uint2*>(dst + 64) = make_uint2(pack2(en[0], en[1]), pack2(en[2], en[3]));
      *reinterpret_cast<uint2*>(dst + 96) = make_uint2(pack2(en[4], en[5]), pack2(en[6], en[7]));
    }
  }
  lds_barrier();
  STAMP(2, 2);
  {
    f32x4 c0 = {0.f, 0.f, 0.f, 0.f}, cz = c0;
    bf16x8 ones;
#pragma unroll
    for (int j = 0; j < 8; ++j) ones[j] = (short)0x3f80;
    const int q = l15 >> 2, p = lane & 3, ft = wid & 3, dt = wid >> 2;
#pragma unroll
    for (int ks = 0; ks < 8; ++ks) {
      const int r0 = ks * 32 + 8 * g + q;
      s16x4 a0 = __builtin_amdgcn_ds_read_tr16_b64_v4i16((LDSP(s16x4))(pk_img + (r0) * 128 + (16 * ft + 4 * p) * 2));
      s16x4 a1 = __builtin_amdgcn_ds_read_tr16_b64_v4i16((LDSP(s16x4))(pk_img + (r0 + 4) * 128 + (16 * ft + 4 * p) * 2));
      bf16x8 afr = __builtin_shufflevector(a0, a1, 0, 1, 2, 3, 4, 5, 6, 7);
      s16x4 b0 = __builtin_amdgcn_ds_read_tr16_b64_v4i16((LDSP(s16x4))(qkv_img + (r0) * QS + 128 + (16 * dt + 4 * p) * 2));
      s16x4 b1 = __builtin_amdgcn_ds_read_tr16_b64_v4i16((LDSP(s16x4))(qkv_img + (r0 + 4) * QS + 128 + (16 * dt + 4 * p) * 2));
      bf16x8 bf0 = __builtin_shufflevector(b0, b1, 0, 1, 2, 3, 4, 5, 6, 7);
      c0 = __builtin_amdgcn_mfma_f32_16x16x32_bf16(afr, bf0, c0, 0, 0, 0);
      cz = __builtin_amdgcn_mfma_f32_16x16x32_bf16(afr, ones, cz, 0, 0, 0);
    }
    float* kd = kvp + ((size_t)(mtile * H + head) * 64) * 32;
#pragma unroll
    for (int r = 0; r < 4; ++r) {
      const int f = 16 * ft + 4 * g + r;
      kd[f * 32 + 16 * dt + l15] = c0[r];
      if (l15 == 0 && dt == 0) zp[(mtile * H + head) * 64 + f] = cz[r];
    }
  }
  STAMP(2, 3);
}

__global__ __launch_bounds__(256) void k_attn_o(const u16* __restrict__ phq, const float* __restrict__ kvp,
                                                const float* __restrict__ zp, u16* __restrict__ o,
                                                const u16* __restrict__ wg, const u16* __restrict__ wu,
                                                const u16* __restrict__ wd) {
  if (blockIdx.x >= 256) {
    const int pb = blockIdx.x - 256;
    prefetch_range(wg, (size_t)D * F * 2, 255, pb);
    prefetch_range(wu, (size_t)D * F * 2, 255, pb);
    prefetch_range(wd, (size_t)D * F * 2, 255, pb);
    return;
  }
  __shared__ __attribute__((aligned(16))) u16 kvT[32][64 + 8];
  __shared__ float zs[64];
  const int head = blockIdx.x & 31, chunk = blockIdx.x >> 5;
  const int t = threadIdx.x, wid = t >> 6, lane = t & 63, l15 = lane & 15, g = lane >> 4;
  {
    const int f = t >> 2, d0 = (t & 3) * 8;
    float s[8];
#pragma unroll
    for (int j = 0; j < 8; ++j) s[j] = 0.f;
#pragma unroll
    for (int p = 0; p < 8; ++p) {
      const float* src = kvp + ((size_t)(p * H + head) * 64 + f) * 32 + d0;
      float4 a = *reinterpret_cast<const float4*>(src);
      float4 b2 = *reinterpret_cast<const float4*>(src + 4);
      s[0] += a.x; s[1] += a.y; s[2] += a.z; s[3] += a.w;
      s[4] += b2.x; s[5] += b2.y; s[6] += b2.z; s[7] += b2.w;
    }
#pragma unroll
    for (int j = 0; j < 8; ++j) kvT[d0 + j][f] = f2bf(s[j]);
    if (t < 64) {
      float z = 0.f;
#pragma unroll
      for (int p = 0; p < 8; ++p) z += zp[(p * H + head) * 64 + t];
      zs[t] = rbf(z);
    }
  }
  __syncthreads();
  bf16x8 af[2][2];
#pragma unroll
  for (int dt = 0; dt < 2; ++dt)
#pragma unroll
    for (int kk = 0; kk < 2; ++kk)
      af[dt][kk] = *reinterpret_cast<const bf16x8*>(&kvT[16 * dt + l15][32 * kk + 8 * g]);
  float zr[2][8];
#pragma unroll
  for (int kk = 0; kk < 2; ++kk)
#pragma unroll
    for (int j = 0; j < 8; ++j) zr[kk][j] = zs[32 * kk + 8 * g + j];
#pragma unroll
  for (int mt = 0; mt < 4; ++mt) {
    const int m = chunk * 256 + wid * 64 + mt * 16 + l15;
    const u16* src = phq + ((size_t)m * H + head) * 64 + 8 * g;
    bf16x8 b0 = *reinterpret_cast<const bf16x8*>(src);
    bf16x8 b1 = *reinterpret_cast<const bf16x8*>(src + 32);
    float den = 0.f;
#pragma unroll
    for (int j = 0; j < 8; ++j) den += bf2f((u16)b0[j]) * zr[0][j] + bf2f((u16)b1[j]) * zr[1][j];
    den += __shfl_xor(den, 16); den += __shfl_xor(den, 32);
    den = rbf(rbf(den) + 1e-6f);
    const float rden = 1.f / den;
#pragma unroll
    for (int dt = 0; dt < 2; ++dt) {
      f32x4 c = {0.f, 0.f, 0.f, 0.f};
      c = __builtin_amdgcn_mfma_f32_16x16x32_bf16(af[dt][0], b0, c, 0, 0, 0);
      c = __builtin_amdgcn_mfma_f32_16x16x32_bf16(af[dt][1], b1, c, 0, 0, 0);
      *reinterpret_cast<uint2*>(o + (size_t)m * D + head * 32 + 16 * dt + 4 * g) =
          make_uint2(pack2(rbf(c[0]) * rden, rbf(c[1]) * rden), pack2(rbf(c[2]) * rden, rbf(c[3]) * rden));
    }
  }
}

__global__ __launch_bounds__(512) void k_oproj(const u16* __restrict__ o, const u16* __restrict__ wo,
                                               const u16* __restrict__ x, const float* __restrict__ modpart,
                                               u16* __restrict__ x1) {
  extern __shared__ __attribute__((aligned(16))) char lds[];
  const int b = blockIdx.x, xcd = b & 7, idx = b >> 3;
  const int mtile = 2 * xcd + (idx & 1), ntile = idx >> 1;
  const int m0 = mtile * 128, n0 = ntile * 64;
  STAMP(4, 0);
  const int t = threadIdx.x, wid = t >> 6, lane = t & 63, l15 = lane & 15, g = lane >> 4;
  f32x4 acc[2][2];
#pragma unroll
  for (int i = 0; i < 2; ++i)
#pragma unroll
    for (int j = 0; j < 2; ++j) acc[i][j] = (f32x4){0.f, 0.f, 0.f, 0.f};
  gemm_core<128, 4, 4, 2, 6>(o, wo, D, m0, [&](int nb) { return n0 + nb * 16; }, lds, acc);
  const int wm = wid >> 1, wn = wid & 1;
  STAMP(4, 1);
#pragma unroll
  for (int nb = 0; nb < 2; ++nb) {
    const int n = n0 + (wn * 2 + nb) * 16 + 4 * g;
    float ga[4];
#pragma unroll
    for (int v = 0; v < 2; ++v) {}
    {
      float4 a = *reinterpret_cast<const float4*>(modpart + 2 * D + n);
#pragma unroll
      for (int p = 1; p < 4; ++p) {
        float4 a2 = *reinterpret_cast<const float4*>(modpart + p * 6144 + 2 * D + n);
        a.x += a2.x; a.y += a2.y; a.z += a2.z; a.w += a2.w;
      }
      ga[0] = rbf(a.x); ga[1] = rbf(a.y); ga[2] = rbf(a.z); ga[3] = rbf(a.w);
    }
#pragma unroll
    for (int mt = 0; mt < 2; ++mt) {
      const int m = m0 + wm * 32 + mt * 16 + l15;
      uint2 xv = *reinterpret_cast<const uint2*>(x + (size_t)m * D + n);
      float r0 = lo16(xv.x) + rbf(ga[0] * rbf(acc[nb][mt][0]));
      float r1 = hi16(xv.x) + rbf(ga[1] * rbf(acc[nb][mt][1]));
      float r2 = lo16(xv.y) + rbf(ga[2] * rbf(acc[nb][mt][2]));
      float r3 = hi16(xv.y) + rbf(ga[3] * rbf(acc[nb][mt][3]));
      *reinterpret_cast<uint2*>(x1 + (size_t)m * D + n) = make_uint2(pack2(r0, r1), pack2(r2, r3));
    }
  }
  STAMP(4, 2);
}

#define RLX_AGENT __ATOMIC_RELAXED, __HIP_MEMORY_SCOPE_AGENT
DI void st_agent(float* p, float v) { __hip_atomic_store(p, v, RLX_AGENT); }
DI float ld_agent(const float* p) { return __hip_atomic_load(p, RLX_AGENT); }
DI void signal(unsigned* ctr) {
  asm volatile("s_waitcnt vmcnt(0)" ::: "memory");
  __syncthreads();
  if (threadIdx.x == 0) __hip_atomic_fetch_add(ctr, 1u, RLX_AGENT);
}
DI void wait_ctr(unsigned* ctr, unsigned target) {
  if (threadIdx.x == 0) {
    for (unsigned spins = 0; spins < 4000000u; ++spins) {
      if (__hip_atomic_load(ctr, RLX_AGENT) >= target) break;
      __builtin_amdgcn_s_sleep(1);
    }
  }
  __syncthreads();
}

DI void ln_row(const u16* xr, int lane, float (&f)[16], float& mean, float& rstd) {
  uint4 v0 = *reinterpret_cast<const uint4*>(xr + lane * 8);
  uint4 v1 = *reinterpret_cast<const uint4*>(xr + 512 + lane * 8);
  f[0] = lo16(v0.x); f[1] = hi16(v0.x); f[2] = lo16(v0.y); f[3] = hi16(v0.y);
  f[4] = lo16(v0.z); f[5] = hi16(v0.z); f[6] = lo16(v0.w); f[7] = hi16(v0.w);
  f[8] = lo16(v1.x); f[9] = hi16(v1.x); f[10] = lo16(v1.y); f[11] = hi16(v1.y);
  f[12] = lo16(v1.z); f[13] = hi16(v1.z); f[14] = lo16(v1.w); f[15] = hi16(v1.w);
  float s = 0.f;
#pragma unroll
  for (int j = 0; j < 16; ++j) s += f[j];
#pragma unroll
  for (int m = 1; m < 64; m <<= 1) s += __shfl_xor(s, m);
  mean = s * (1.f / D);
  float q = 0.f;
#pragma unroll
  for (int j = 0; j < 16; ++j) { float d = f[j] - mean; q += d * d; }
#pragma unroll
  for (int m = 1; m < 64; m <<= 1) q += __shfl_xor(q, m);
  rstd = rsqrtf(q * (1.f / D) + 1e-5f);
}

__global__ __launch_bounds__(256) void k_ln2(const u16* __restrict__ x1, const float* __restrict__ modpart,
                                             const u16* __restrict__ wr, u16* __restrict__ h2,
                                             u16* __restrict__ out, float* __restrict__ r) {
  __shared__ float sc[D], sh[D], gbv[D];
  const int t = threadIdx.x, wid = t >> 6, lane = t & 63;
  uint4 xv[2][2];
#pragma unroll
  for (int q2 = 0; q2 < 2; ++q2) {
    const u16* xr = x1 + (size_t)(blockIdx.x * 8 + wid * 2 + q2) * D;
    xv[q2][0] = *reinterpret_cast<const uint4*>(xr + lane * 8);
    xv[q2][1] = *reinterpret_cast<const uint4*>(xr + 512 + lane * 8);
  }
  uint4 w0 = *reinterpret_cast<const uint4*>(wr + lane * 8);
  uint4 w1 = *reinterpret_cast<const uint4*>(wr + 512 + lane * 8);
  {
    const int c4 = t * 4;
    float4 a = *reinterpret_cast<const float4*>(modpart + 3 * D + c4);
    float4 bb = *reinterpret_cast<const float4*>(modpart + 4 * D + c4);
    float4 c = *reinterpret_cast<const float4*>(modpart + 5 * D + c4);
#pragma unroll
    for (int p = 1; p < 4; ++p) {
      float4 a2 = *reinterpret_cast<const float4*>(modpart + p * 6144 + 3 * D + c4);
      float4 b2 = *reinterpret_cast<const float4*>(modpart + p * 6144 + 4 * D + c4);
      float4 c2 = *reinterpret_cast<const float4*>(modpart + p * 6144 + 5 * D + c4);
      a.x += a2.x; a.y += a2.y; a.z += a2.z; a.w += a2.w;
      bb.x += b2.x; bb.y += b2.y; bb.z += b2.z; bb.w += b2.w;
      c.x += c2.x; c.y += c2.y; c.z += c2.z; c.w += c2.w;
    }
    sh[c4] = rbf(a.x); sh[c4 + 1] = rbf(a.y); sh[c4 + 2] = rbf(a.z); sh[c4 + 3] = rbf(a.w);
    sc[c4] = rbf(1.f + rbf(bb.x)); sc[c4 + 1] = rbf(1.f + rbf(bb.y));
    sc[c4 + 2] = rbf(1.f + rbf(bb.z)); sc[c4 + 3] = rbf(1.f + rbf(bb.w));
    gbv[c4] = rbf(c.x); gbv[c4 + 1] = rbf(c.y); gbv[c4 + 2] = rbf(c.z); gbv[c4 + 3] = rbf(c.w);
  }
  __syncthreads();
  float wf[16];
  wf[0] = lo16(w0.x); wf[1] = hi16(w0.x); wf[2] = lo16(w0.y); wf[3] = hi16(w0.y);
  wf[4] = lo16(w0.z); wf[5] = hi16(w0.z); wf[6] = lo16(w0.w); wf[7] = hi16(w0.w);
  wf[8] = lo16(w1.x); wf[9] = hi16(w1.x); wf[10] = lo16(w1.y); wf[11] = hi16(w1.y);
  wf[12] = lo16(w1.z); wf[13] = hi16(w1.z); wf[14] = lo16(w1.w); wf[15] = hi16(w1.w);
#pragma unroll
  for (int q2 = 0; q2 < 2; ++q2) {
    const int row = blockIdx.x * 8 + wid * 2 + q2;
    const uint4 v0 = xv[q2][0], v1 = xv[q2][1];
    float f[16];
    f[0] = lo16(v0.x); f[1] = hi16(v0.x); f[2] = lo16(v0.y); f[3] = hi16(v0.y);
    f[4] = lo16(v0.z); f[5] = hi16(v0.z); f[6] = lo16(v0.w); f[7] = hi16(v0.w);
    f[8] = lo16(v1.x); f[9] = hi16(v1.x); f[10] = lo16(v1.y); f[11] = hi16(v1.y);
    f[12] = lo16(v1.z); f[13] = hi16(v1.z); f[14] = lo16(v1.w); f[15] = hi16(v1.w);
    float s = 0.f;
#pragma unroll
    for (int j = 0; j < 16; ++j) s += f[j];
#pragma unroll
    for (int m = 1; m < 64; m <<= 1) s += __shfl_xor(s, m);
    const float mean = s * (1.f / D);
    float q = 0.f;
#pragma unroll
    for (int j = 0; j < 16; ++j) { float d = f[j] - mean; q += d * d; }
#pragma unroll
    for (int m = 1; m < 64; m <<= 1) q += __shfl_xor(q, m);
    const float rstd = rsqrtf(q * (1.f / D) + 1e-5f);
    unsigned ho[8], oo[8];
    float dot = 0.f;
#pragma unroll
    for (int j = 0; j < 16; j += 2) {
      const int c0 = (j < 8) ? lane * 8 + j : 512 + lane * 8 + (j - 8);
      float a = rbf(rbf((f[j] - mean) * rstd) * sc[c0] + sh[c0]);
      float bq = rbf(rbf((f[j + 1] - mean) * rstd) * sc[c0 + 1] + sh[c0 + 1]);
      dot += a * wf[j] + bq * wf[j + 1];
      ho[j >> 1] = pack2(a, bq);
      oo[j >> 1] = pack2(f[j] + rbf(gbv[c0] * a), f[j + 1] + rbf(gbv[c0 + 1] * bq));
    }
#pragma unroll
    for (int m = 1; m < 64; m <<= 1) dot += __shfl_xor(dot, m);
    u16* hr = h2 + (size_t)row * D;
    *reinterpret_cast<uint4*>(hr + lane * 8) = make_uint4(ho[0], ho[1], ho[2], ho[3]);
    *reinterpret_cast<uint4*>(hr + 512 + lane * 8) = make_uint4(ho[4], ho[5], ho[6], ho[7]);
    u16* orow = out + (size_t)row * D;
    *reinterpret_cast<uint4*>(orow + lane * 8) = make_uint4(oo[0], oo[1], oo[2], oo[3]);
    *reinterpret_cast<uint4*>(orow + 512 + lane * 8) = make_uint4(oo[4], oo[5], oo[6], oo[7]);
    if (lane == 0) r[row] = rbf(dot);
  }
}

__global__ __launch_bounds__(256) void k_moe(const float* __restrict__ r, const u16* __restrict__ h2,
                                             const u16* __restrict__ x1, const float* __restrict__ modpart,
                                             const u16* __restrict__ wg, const u16* __restrict__ wu,
                                             const u16* __restrict__ wd, u16* __restrict__ out,
                                             float* __restrict__ gup, float* __restrict__ dpart,
                                             unsigned* __restrict__ ctr) {
  __shared__ float red[32][65];
  __shared__ float hs[2][256];
  __shared__ float sm_v[8]; __shared__ int sm_i[8];
  __shared__ int s_last;
  const int b = blockIdx.x, t = threadIdx.x, wid = t >> 6, lane = t & 63;
  const int cq = t & 7, rr = t >> 3;
  const int gcc = b % 56, gks = b / 56;
  const int dcc = b & 15, dks = b >> 4;
  float rv8[8];
#pragma unroll
  for (int i = 0; i < 8; ++i) rv8[i] = r[t + i * 256];
  uint4 wa[8], wb[8];
#pragma unroll
  for (int i = 0; i < 8; ++i) {
    const int k = gks * 256 + i * 32 + rr;
    wa[i] = *reinterpret_cast<const uint4*>(wg + (size_t)k * F + gcc * 64 + cq * 8);
    wb[i] = *reinterpret_cast<const uint4*>(wu + (size_t)k * F + gcc * 64 + cq * 8);
  }
  int i0, i1; float r0, r1;
  {
    float b0 = -INFINITY, b1 = -INFINITY; int j0 = 0x7fffffff, j1 = 0x7fffffff;
#pragma unroll
    for (int i = 0; i < 8; ++i) {
      const int idx = t + i * 256;
      const float v = rv8[i];
      if (v > b0 || (v == b0 && idx < j0)) { b1 = b0; j1 = j0; b0 = v; j0 = idx; }
      else if (v > b1 || (v == b1 && idx < j1)) { b1 = v; j1 = idx; }
    }
#pragma unroll
    for (int m = 1; m < 64; m <<= 1) {
      float o0 = __shfl_xor(b0, m), o1 = __shfl_xor(b1, m);
      int p0 = __shfl_xor(j0, m), p1 = __shfl_xor(j1, m);
      if (o0 > b0 || (o0 == b0 && p0 < j0)) {
        if (b0 > o1 || (b0 == o1 && j0 < p1)) { b1 = b0; j1 = j0; } else { b1 = o1; j1 = p1; }
        b0 = o0; j0 = p0;
      } else {
        if (o0 > b1 || (o0 == b1 && p0 < j1)) { b1 = o0; j1 = p0; }
      }
    }
    if (lane == 0) { sm_v[wid * 2] = b0; sm_v[wid * 2 + 1] = b1; sm_i[wid * 2] = j0; sm_i[wid * 2 + 1] = j1; }
    __syncthreads();
    b0 = sm_v[0]; b1 = sm_v[1]; j0 = sm_i[0]; j1 = sm_i[1];
#pragma unroll
    for (int w = 1; w < 4; ++w) {
      float o0 = sm_v[w * 2], o1 = sm_v[w * 2 + 1];
      int p0 = sm_i[w * 2], p1 = sm_i[w * 2 + 1];
      if (o0 > b0 || (o0 == b0 && p0 < j0)) {
        if (b0 > o1 || (b0 == o1 && j0 < p1)) { b1 = b0; j1 = j0; } else { b1 = o1; j1 = p1; }
        b0 = o0; j0 = p0;
      } else {
        if (o0 > b1 || (o0 == b1 && p0 < j1)) { b1 = o0; j1 = p0; }
      }
    }
    i0 = j0; i1 = j1; r0 = b0; r1 = b1;
  }
  float hv0[8], hv1[8];
#pragma unroll
  for (int i = 0; i < 8; ++i) {
    const int k = gks * 256 + i * 32 + rr;
    hv0[i] = bf2f(h2[(size_t)i0 * D + k]); hv1[i] = bf2f(h2[(size_t)i1 * D + k]);
  }
  uint4 wdv[8];
#pragma unroll
  for (int i = 0; i < 8; ++i) {
    const int k = dks * 256 + i * 32 + rr;
    wdv[i] = *reinterpret_cast<const uint4*>(wd + (size_t)k * D + dcc * 64 + cq * 8);
  }
  {
    float acc[4][8];
#pragma unroll
    for (int a = 0; a < 4; ++a)
#pragma unroll
      for (int j = 0; j < 8; ++j) acc[a][j] = 0.f;
#pragma unroll
    for (int i = 0; i < 8; ++i) {
      const float h0 = hv0[i], h1v = hv1[i];
      const unsigned wv[4] = {wa[i].x, wa[i].y, wa[i].z, wa[i].w};
      const unsigned uv[4] = {wb[i].x, wb[i].y, wb[i].z, wb[i].w};
#pragma unroll
      for (int j = 0; j < 4; ++j) {
        acc[0][2 * j] += h0 * lo16(wv[j]);  acc[0][2 * j + 1] += h0 * hi16(wv[j]);
        acc[1][2 * j] += h1v * lo16(wv[j]); acc[1][2 * j + 1] += h1v * hi16(wv[j]);
        acc[2][2 * j] += h0 * lo16(uv[j]);  acc[2][2 * j + 1] += h0 * hi16(uv[j]);
        acc[3][2 * j] += h1v * lo16(uv[j]); acc[3][2 * j + 1] += h1v * hi16(uv[j]);
      }
    }
#pragma unroll
    for (int a = 0; a < 4; ++a) {
      __syncthreads();
#pragma unroll
      for (int j = 0; j < 8; ++j) red[rr][cq * 8 + j] = acc[a][j];
      __syncthreads();
      if (t < 64) {
        float sum = 0.f;
#pragma unroll
        for (int i = 0; i < 32; ++i) sum += red[i][t];
        st_agent(gup + (size_t)(gks * 4 + a) * F + gcc * 64 + t, sum);
      }
    }
  }
  signal(ctr + 16 + (gcc >> 2));
  wait_ctr(ctr + 16 + dks, 16);
  {
    const int k = dks * 256 + t;
#pragma unroll
    for (int tok = 0; tok < 2; ++tok) {
      float gsum = 0.f, usum = 0.f;
#pragma unroll
      for (int p = 0; p < 4; ++p) {
        gsum += ld_agent(gup + (size_t)(p * 4 + tok) * F + k);
        usum += ld_agent(gup + (size_t)(p * 4 + 2 + tok) * F + k);
      }
      gsum = rbf(gsum); usum = rbf(usum);
      const float sg = rbf(gsum / (1.f + __expf(-gsum)));
      hs[tok][t] = rbf(sg * usum);
    }
  }
  __syncthreads();
  {
    float ac2[2][8];
#pragma unroll
    for (int a = 0; a < 2; ++a)
#pragma unroll
      for (int j = 0; j < 8; ++j) ac2[a][j] = 0.f;
#pragma unroll
    for (int i = 0; i < 8; ++i) {
      const int kl = i * 32 + rr;
      const float h0 = hs[0][kl], h1v = hs[1][kl];
      const unsigned wv[4] = {wdv[i].x, wdv[i].y, wdv[i].z, wdv[i].w};
#pragma unroll
      for (int j = 0; j < 4; ++j) {
        ac2[0][2 * j] += h0 * lo16(wv[j]);  ac2[0][2 * j + 1] += h0 * hi16(wv[j]);
        ac2[1][2 * j] += h1v * lo16(wv[j]); ac2[1][2 * j + 1] += h1v * hi16(wv[j]);
      }
    }
#pragma unroll
    for (int a = 0; a < 2; ++a) {
      __syncthreads();
#pragma unroll
      for (int j = 0; j < 8; ++j) red[rr][cq * 8 + j] = ac2[a][j];
      __syncthreads();
      if (t < 64) {
        float sum = 0.f;
#pragma unroll
        for (int i = 0; i < 32; ++i) sum += red[i][t];
        st_agent(dpart + (size_t)(dks * 2 + a) * D + dcc * 64 + t, sum);
      }
    }
  }
  asm volatile("s_waitcnt vmcnt(0)" ::: "memory");
  __syncthreads();
  if (t == 0) s_last = (__hip_atomic_fetch_add(ctr + 32 + dcc, 1u, RLX_AGENT) == 13u);
  __syncthreads();
  if (s_last && t < 128) {
    const int tok = t >> 6, n = dcc * 64 + (t & 63);
    const int row = tok ? i1 : i0;
    const float rv = tok ? r1 : r0;
    float y = 0.f;
#pragma unroll
    for (int p = 0; p < 14; ++p) y += ld_agent(dpart + (size_t)(p * 2 + tok) * D + n);
    const float sig = rbf(1.f / (1.f + __expf(-rv)));
    y = rbf(rbf(y) * sig);
    float gb = 0.f;
#pragma unroll
    for (int p = 0; p < 4; ++p) gb += modpart[p * 6144 + 5 * D + n];
    gb = rbf(gb);
    const float hv = bf2f(h2[(size_t)row * D + n]);
    const float xv = bf2f(x1[(size_t)row * D + n]);
    out[(size_t)row * D + n] = f2bf(xv + rbf(gb * rbf(hv + y)));
  }
}

extern "C" void kernel_launch(void* const* d_in, const int* in_sizes, int n_in, void* d_out, int out_size,
                              void* d_ws, size_t ws_size, hipStream_t stream) {
  const u16* x    = (const u16*)d_in[0];
  const u16* wqkv = (const u16*)d_in[1];
  const u16* wphi = (const u16*)d_in[2];
  const u16* wo   = (const u16*)d_in[3];
  const u16* w_r  = (const u16*)d_in[5];
  const u16* wg   = (const u16*)d_in[6];
  const u16* wu   = (const u16*)d_in[7];
  const u16* wd   = (const u16*)d_in[8];
  const u16* c    = (const u16*)d_in[9];
  const u16* wmod = (const u16*)d_in[10];
  u16* out = (u16*)d_out;

  char* ws = (char*)d_ws;
  size_t off = 0;
  auto alloc = [&](size_t bytes) { char* p = ws + off; off += (bytes + 255) & ~(size_t)255; return p; };
  float* modpart = (float*)alloc(4 * 6144 * 4);
  u16* h1  = (u16*)alloc((size_t)S * D * 2);
  u16* phq = (u16*)alloc((size_t)S * H * 64 * 2);
  float* kvp = (float*)alloc((size_t)8 * H * 64 * 32 * 4);
  float* zp  = (float*)alloc((size_t)8 * H * 64 * 4);
  u16* o   = (u16*)alloc((size_t)S * D * 2);
  u16* x1  = (u16*)alloc((size_t)S * D * 2);
  u16* h2  = (u16*)alloc((size_t)S * D * 2);
  float* r = (float*)alloc(S * 4);
  float* gup = (float*)alloc((size_t)16 * F * 4);
  float* dpart = (float*)alloc((size_t)28 * D * 4);
  unsigned* ctr = (unsigned*)alloc(256);

  k_mod<<<384, 256, 0, stream>>>(c, wmod, modpart, ctr);
  k_ln1<<<256, 256, 0, stream>>>(x, modpart, h1);
  constexpr int LDS_QKV = 3 * (256 * 128 + 64 * 192);
  constexpr int LDS_OP  = 6 * (128 * 128 + 64 * 128);
  hipFuncSetAttribute((const void*)k_qkv, hipFuncAttributeMaxDynamicSharedMemorySize, LDS_QKV);
  hipFuncSetAttribute((const void*)k_oproj, hipFuncAttributeMaxDynamicSharedMemorySize, LDS_OP);
  k_qkv<<<256, 512, LDS_QKV, stream>>>(h1, wqkv, wphi, phq, kvp, zp);
  k_attn_o<<<256 + PFB, 256, 0, stream>>>(phq, kvp, zp, o, wg, wu, wd);
  k_oproj<<<256, 512, LDS_OP, stream>>>(o, wo, x, modpart, x1);
  k_ln2<<<256, 256, 0, stream>>>(x1, modpart, w_r, h2, out, r);
  k_moe<<<224, 256, 0, stream>>>(r, h2, x1, modpart, wg, wu, wd, out, gup, dpart, ctr);
}
```

```cpp
#include <hip/hip_runtime.h>
#include <hip/hip_bf16.h>
#include <stdint.h>
#define PFB 0
#define QMAP 0
#define OPST 3

constexpr int S  = 2048;
constexpr int D  = 1024;
constexpr int H  = 32;
constexpr int F  = 3584;

typedef short  s16x4  __attribute__((ext_vector_type(4)));
typedef short  bf16x8 __attribute__((ext_vector_type(8)));
typedef float  f32x4  __attribute__((ext_vector_type(4)));
typedef unsigned short u16;
#define LDSP(T) __attribute__((address_space(3))) T*
#define DI __device__ __forceinline__
#define STAMP(k, i) do {} while (0)

DI float bf2f(u16 x) { return __uint_as_float(((unsigned)x) << 16); }
typedef __bf16 bf16v2 __attribute__((ext_vector_type(2)));
typedef float f32v2 __attribute__((ext_vector_type(2)));
DI u16 f2bf(float f) { __bf16 b = (__bf16)f; return __builtin_bit_cast(u16, b); }
DI float rbf(float f) { return (float)(__bf16)f; }
DI unsigned pack2(float a, float b) {
  f32v2 v = {a, b};
  return __builtin_bit_cast(unsigned, __builtin_convertvector(v, bf16v2));
}
DI float lo16(unsigned u) { return __uint_as_float(u << 16); }
DI float hi16(unsigned u) { return __uint_as_float(u & 0xffff0000u); }

__global__ __launch_bounds__(256) void k_mod(const u16* __restrict__ c, const u16* __restrict__ wmod,
                                             float* __restrict__ part, unsigned* __restrict__ ctr) {
  if (blockIdx.x == 0 && threadIdx.x < 64) ctr[threadIdx.x] = 0u;
  __shared__ float red[32][65];
  const int cc = blockIdx.x % 96, ks = blockIdx.x / 96;
  const int t = threadIdx.x, cq = t & 7, rr = t >> 3;
  const int col0 = cc * 64 + cq * 8;
  uint4 w[8];
#pragma unroll
  for (int i = 0; i < 8; ++i) {
    const int k = ks * 256 + i * 32 + rr;
    w[i] = *reinterpret_cast<const uint4*>(wmod + (size_t)k * 6144 + col0);
  }
  float acc[8];
#pragma unroll
  for (int j = 0; j < 8; ++j) acc[j] = 0.f;
#pragma unroll
  for (int i = 0; i < 8; ++i) {
    const int k = ks * 256 + i * 32 + rr;
    const float cv = bf2f(c[k]);
    const float s = rbf(cv / (1.f + __expf(-cv)));
    acc[0] += s * lo16(w[i].x); acc[1] += s * hi16(w[i].x);
    acc[2] += s * lo16(w[i].y); acc[3] += s * hi16(w[i].y);
    acc[4] += s * lo16(w[i].z); acc[5] += s * hi16(w[i].z);
    acc[6] += s * lo16(w[i].w); acc[7] += s * hi16(w[i].w);
  }
#pragma unroll
  for (int j = 0; j < 8; ++j) red[rr][cq * 8 + j] = acc[j];
  __syncthreads();
  if (t < 64) {
    float s = 0.f;
#pragma unroll
    for (int i = 0; i < 32; ++i) s += red[i][t];
    part[ks * 6144 + cc * 64 + t] = s;
  }
}

__global__ __launch_bounds__(256) void k_ln1(const u16* __restrict__ x, const float* __restrict__ modpart,
                                             u16* __restrict__ h) {
  __shared__ float sc[D], sh[D];
  const int t = threadIdx.x;
  {
    const int c4 = t * 4;
    float4 a = *reinterpret_cast<const float4*>(modpart + c4);
    float4 b = *reinterpret_cast<const float4*>(modpart + D + c4);
#pragma unroll
    for (int p = 1; p < 4; ++p) {
      float4 a2 = *reinterpret_cast<const float4*>(modpart + p * 6144 + c4);
      float4 b2 = *reinterpret_cast<const float4*>(modpart + p * 6144 + D + c4);
      a.x += a2.x; a.y += a2.y; a.z += a2.z; a.w += a2.w;
      b.x += b2.x; b.y += b2.y; b.z += b2.z; b.w += b2.w;
    }
    sh[c4] = rbf(a.x); sh[c4 + 1] = rbf(a.y); sh[c4 + 2] = rbf(a.z); sh[c4 + 3] = rbf(a.w);
    sc[c4] = rbf(1.f + rbf(b.x)); sc[c4 + 1] = rbf(1.f + rbf(b.y));
    sc[c4 + 2] = rbf(1.f + rbf(b.z)); sc[c4 + 3] = rbf(1.f + rbf(b.w));
  }
  __syncthreads();
  const int wid = t >> 6, lane = t & 63;
#pragma unroll
  for (int rr = 0; rr < 2; ++rr) {
    const int row = blockIdx.x * 8 + wid * 2 + rr;
    const u16* xr = x + (size_t)row * D;
    uint4 v0 = *reinterpret_cast<const uint4*>(xr + lane * 8);
    uint4 v1 = *reinterpret_cast<const uint4*>(xr + 512 + lane * 8);
    float f[16];
    f[0] = lo16(v0.x); f[1] = hi16(v0.x); f[2] = lo16(v0.y); f[3] = hi16(v0.y);
    f[4] = lo16(v0.z); f[5] = hi16(v0.z); f[6] = lo16(v0.w); f[7] = hi16(v0.w);
    f[8] = lo16(v1.x); f[9] = hi16(v1.x); f[10] = lo16(v1.y); f[11] = hi16(v1.y);
    f[12] = lo16(v1.z); f[13] = hi16(v1.z); f[14] = lo16(v1.w); f[15] = hi16(v1.w);
    float s = 0.f;
#pragma unroll
    for (int j = 0; j < 16; ++j) s += f[j];
#pragma unroll
    for (int m = 1; m < 64; m <<= 1) s += __shfl_xor(s, m);
    const float mean = s * (1.f / D);
    float q = 0.f;
#pragma unroll
    for (int j = 0; j < 16; ++j) { float d = f[j] - mean; q += d * d; }
#pragma unroll
    for (int m = 1; m < 64; m <<= 1) q += __shfl_xor(q, m);
    const float rstd = rsqrtf(q * (1.f / D) + 1e-5f);
    unsigned o[8];
#pragma unroll
    for (int j = 0; j < 16; j += 2) {
      const int c0 = (j < 8) ? lane * 8 + j : 512 + lane * 8 + (j - 8);
      float a = rbf((f[j] - mean) * rstd) * sc[c0] + sh[c0];
      float b = rbf((f[j + 1] - mean) * rstd) * sc[c0 + 1] + sh[c0 + 1];
      o[j >> 1] = pack2(a, b);
    }
    u16* hr = h + (size_t)row * D;
    *reinterpret_cast<uint4*>(hr + lane * 8) = make_uint4(o[0], o[1], o[2], o[3]);
    *reinterpret_cast<uint4*>(hr + 512 + lane * 8) = make_uint4(o[4], o[5], o[6], o[7]);
  }
}

template <int UK> DI int xswz(int row, int c) {
  if (UK == 64) return c ^ ((row >> 1) & 7);
  else return c ^ ((0x78 >> (2 * ((row >> 2) & 3))) & 3);
}
template <int PPR> DI int wperm(int k, int j) {
  if (PPR == 12) { int v = j + 2 * ((k >> 3) & 1); return v >= 12 ? v - 12 : v; }
  else return j ^ (2 * (((k >> 1) & 1) | (((k >> 3) & 1) << 1)));
}
template <int PPR> DI int wperm_inv(int k, int pos) {
  if (PPR == 12) { int v = pos - 2 * ((k >> 3) & 1); return v < 0 ? v + 12 : v; }
  else return pos ^ (2 * (((k >> 1) & 1) | (((k >> 3) & 1) << 1)));
}

template <int BM, int NB, int WM, int WN, int NST, typename ColMap>
DI void gemm_core(const u16* __restrict__ X, const u16* __restrict__ W, int ldw, int m0, ColMap colmap, char* lds,
                  f32x4 (&acc)[NB / WN][BM / WM / 16]) {
  constexpr int UK = 64;
  constexpr int MT = BM / WM / 16, NW = NB / WN;
  constexpr int XRB = UK * 2, CPR = UK / 8;
  constexpr int PPR = NB * 2, WRB = NB * 32;
  constexpr int XB = BM * XRB, WB = UK * WRB, UB = XB + WB;
  constexpr int XT = XB / 1024, WT = WB / 1024;
  constexpr int XI = XT / 8, WI = (WT + 7) / 8, WMIN = WT / 8;
  constexpr int NU = D / UK;
  static_assert(XT % 8 == 0, "");
  const int t = threadIdx.x, wid = __builtin_amdgcn_readfirstlane(t >> 6), lane = t & 63;
  const u16* xsrc[XI];
#pragma unroll
  for (int i = 0; i < XI; ++i) {
    const int p = (wid * XI + i) * 64 + lane;
    const int row = p / CPR, c = xswz<UK>(row, p % CPR);
    xsrc[i] = X + (size_t)(m0 + row) * D + c * 8;
  }
  const u16* wsrc[WI];
#pragma unroll
  for (int i = 0; i < WI; ++i) {
    const int p = (wid + i * 8) * 64 + lane;
    const int k = (p / PPR) % UK, j = wperm_inv<PPR>(k, p % PPR);
    wsrc[i] = W + (size_t)k * ldw + colmap(j >> 1) + 8 * (j & 1);
  }
  auto issue = [&](int u) {
    char* base = lds + (u % NST) * UB;
#pragma unroll
    for (int i = 0; i < XI; ++i)
      __builtin_amdgcn_global_load_lds((const unsigned*)(xsrc[i] + u * UK), (LDSP(unsigned))(base + (wid * XI + i) * 1024), 16, 0, 0);
#pragma unroll
    for (int i = 0; i < WI; ++i)
      if (wid + i * 8 < WT)
        __builtin_amdgcn_global_load_lds((const unsigned*)(wsrc[i] + (size_t)u * UK * ldw), (LDSP(unsigned))(base + XB + (wid + i * 8) * 1024), 16, 0, 0);
  };
  const int wm = wid / WN, wn = wid % WN;
  const int l15 = lane & 15, g = lane >> 4, q = l15 >> 2, p = lane & 3;
  int xoff[MT][2], woff[NW][2][2];
#pragma unroll
  for (int kk = 0; kk < 2; ++kk) {
#pragma unroll
    for (int mt = 0; mt < MT; ++mt) {
      const int row = wm * (BM / WM) + mt * 16 + l15;
      xoff[mt][kk] = row * XRB + xswz<UK>(row, kk * 4 + g) * 16;
    }
#pragma unroll
    for (int nb = 0; nb < NW; ++nb)
#pragma unroll
      for (int tt = 0; tt < 2; ++tt) {
        const int k = kk * 32 + 8 * g + 4 * tt + q;
        woff[nb][kk][tt] = XB + k * WRB + wperm<PPR>(k, 2 * (wn * NW + nb) + (p >> 1)) * 16 + 8 * (p & 1);
      }
  }
  bf16x8 fa[2][NW], fb[2][MT];
  auto rd = [&](int kk, int u) {
    const char* base = lds + (u % NST) * UB;
#pragma unroll
    for (int mt = 0; mt < MT; ++mt) fb[kk][mt] = *reinterpret_cast<const bf16x8*>(base + xoff[mt][kk]);
#pragma unroll
    for (int nb = 0; nb < NW; ++nb) {
      s16x4 a0 = __builtin_amdgcn_ds_read_tr16_b64_v4i16((LDSP(s16x4))(base + woff[nb][kk][0]));
      s16x4 a1 = __builtin_amdgcn_ds_read_tr16_b64_v4i16((LDSP(s16x4))(base + woff[nb][kk][1]));
      fa[kk][nb] = __builtin_shufflevector(a0, a1, 0, 1, 2, 3, 4, 5, 6, 7);
    }
  };
  auto mm = [&](int kk) {
#pragma unroll
    for (int nb = 0; nb < NW; ++nb)
#pragma unroll
      for (int mt = 0; mt < MT; ++mt)
        acc[nb][mt] = __builtin_amdgcn_mfma_f32_16x16x32_bf16(fa[kk][nb], fb[kk][mt], acc[nb][mt], 0, 0, 0);
  };
#pragma unroll
  for (int u = 0; u < NST - 1; ++u) issue(u);
  asm volatile("s_waitcnt vmcnt(%0)" ::"i"((NST - 2) * (XI + WMIN)) : "memory");
  __builtin_amdgcn_s_barrier();
  rd(0, 0);
  for (int u = 0; u < NU; ++u) {
    if (u + NST - 1 < NU) issue(u + NST - 1);
    rd(1, u);
    mm(0);
    if (u + NST - 1 < NU) { asm volatile("s_waitcnt vmcnt(%0) lgkmcnt(0)" ::"i"((NST - 2) * (XI + WMIN)) : "memory"); }
    else                  { asm volatile("s_waitcnt vmcnt(0) lgkmcnt(0)" ::: "memory"); }
    __builtin_amdgcn_s_barrier();
    if (u + 1 < NU) rd(0, u + 1);
    mm(1);
  }
  __syncthreads();
}

DI void prefetch_range(const void* base, size_t bytes, int nblk, int blk) {
  const size_t per = (bytes / 16 + nblk - 1) / nblk;
  const uint4* p = (const uint4*)base + per * blk;
  const size_t tot = bytes / 16;
  unsigned acc = 0;
  for (size_t i = threadIdx.x; i < per; i += 8 * blockDim.x) {
    uint4 v[8];
#pragma unroll
    for (int j = 0; j < 8; ++j) { size_t idx = i + j * blockDim.x; v[j] = (idx < per && per * blk + idx < tot) ? p[idx] : make_uint4(0, 0, 0, 0); }
#pragma unroll
    for (int j = 0; j < 8; ++j) acc ^= v[j].x ^ v[j].y ^ v[j].z ^ v[j].w;
  }
  asm volatile("" ::"v"(acc));
}

DI void lds_barrier() { asm volatile("s_waitcnt lgkmcnt(0)" ::: "memory"); __builtin_amdgcn_s_barrier(); }

DI float red_x16(float x, bool mx) {
  auto r = __builtin_amdgcn_permlane16_swap(__float_as_uint(x), __float_as_uint(x), false, false);
  float a = __uint_as_float(r[0]), b = __uint_as_float(r[1]);
  return mx ? fmaxf(a, b) : a + b;
}
DI float red_x32(float x, bool mx) {
  auto r = __builtin_amdgcn_permlane32_swap(__float_as_uint(x), __float_as_uint(x), false, false);
  float a = __uint_as_float(r[0]), b = __uint_as_float(r[1]);
  return mx ? fmaxf(a, b) : a + b;
}

DI void softmax_pm(const f32x4& p0, const f32x4& p1, float (&ep)[8], float (&en)[8]) {
  float v[8] = {rbf(p0[0]), rbf(p0[1]), rbf(p0[2]), rbf(p0[3]), rbf(p1[0]), rbf(p1[1]), rbf(p1[2]), rbf(p1[3])};
  float mx = v[0], mn = v[0];
#pragma unroll
  for (int j = 1; j < 8; ++j) { mx = fmaxf(mx, v[j]); mn = fminf(mn, v[j]); }
  float nmn = -mn;
  mx = red_x32(red_x16(mx, true), true);
  nmn = red_x32(red_x16(nmn, true), true);
  float sp = 0.f, sn = 0.f;
#pragma unroll
  for (int j = 0; j < 8; ++j) {
    ep[j] = __expf(v[j] - mx); en[j] = __expf(-nmn - v[j]);
    sp += ep[j]; sn += en[j];
  }
  sp = red_x32(red_x16(sp, false), false);
  sn = red_x32(red_x16(sn, false), false);
  const float rp = __builtin_amdgcn_rcpf(sp), rn = __builtin_amdgcn_rcpf(sn);
#pragma unroll
  for (int j = 0; j < 8; ++j) { ep[j] *= rp; en[j] *= rn; }
}

__global__ __launch_bounds__(512) void k_qkv(const u16* __restrict__ h1, const u16* __restrict__ wqkv,
                                             const u16* __restrict__ wphi, u16* __restrict__ phq,
                                             float* __restrict__ kvp, float* __restrict__ zp) {
  extern __shared__ __attribute__((aligned(16))) char lds[];
  const int b = blockIdx.x, xcd = b & 7, idx = b >> 3;
  const int mtile = 2 * (xcd & 3) + (idx & 1), head = 16 * (xcd >> 2) + (idx >> 1);
  const int m0 = mtile * 256;
  STAMP(2, 0);
  const int t = threadIdx.x, wid = t >> 6, lane = t & 63, l15 = lane & 15, g = lane >> 4;
  bf16x8 aphi[2];
#pragma unroll
  for (int ft = 0; ft < 2; ++ft)
#pragma unroll
    for (int j = 0; j < 8; ++j) aphi[ft][j] = (short)wphi[(head * 32 + 8 * g + j) * 32 + 16 * ft + l15];
  f32x4 acc[3][4];
#pragma unroll
  for (int i = 0; i < 3; ++i)
#pragma unroll
    for (int j = 0; j < 4; ++j) acc[i][j] = (f32x4){0.f, 0.f, 0.f, 0.f};
  gemm_core<256, 6, 4, 2, 3>(h1, wqkv, 3 * D, m0, [&](int nb) { return (nb >> 1) * D + head * 32 + (nb & 1) * 16; }, lds, acc);
  STAMP(2, 1);
  constexpr int QS = 208;
  char* qkv_img = lds;
  char* pk_img = lds + 256 * QS;
  {
    const int wm = wid >> 1, wn = wid & 1;
#pragma unroll
    for (int nb = 0; nb < 3; ++nb)
#pragma unroll
      for (int mt = 0; mt < 4; ++mt) {
        const int m = wm * 64 + mt * 16 + l15, n = (wn * 3 + nb) * 16 + 4 * g;
        *reinterpret_cast<uint2*>(qkv_img + m * QS + n * 2) =
            make_uint2(pack2(acc[nb][mt][0], acc[nb][mt][1]), pack2(acc[nb][mt][2], acc[nb][mt][3]));
      }
  }
  lds_barrier();
#pragma unroll
  for (int mt = 0; mt < 2; ++mt) {
    const int m = wid * 32 + mt * 16 + l15;
    const f32x4 z4 = {0.f, 0.f, 0.f, 0.f};
    {
      bf16x8 bq = *reinterpret_cast<const bf16x8*>(qkv_img + m * QS + 16 * g);
      f32x4 p0 = __builtin_amdgcn_mfma_f32_16x16x32_bf16(aphi[0], bq, z4, 0, 0, 0);
      f32x4 p1 = __builtin_amdgcn_mfma_f32_16x16x32_bf16(aphi[1], bq, z4, 0, 0, 0);
      float ep[8], en[8];
      softmax_pm(p0, p1, ep, en);
      u16* dst = phq + ((size_t)(m0 + m) * H + head) * 64 + 4 * g;
      *reinterpret_cast<uint2*>(dst)      = make_uint2(pack2(ep[0], ep[1]), pack2(ep[2], ep[3]));
      *reinterpret_cast<uint2*>(dst + 16) = make_uint2(pack2(ep[4], ep[5]), pack2(ep[6], ep[7]));
      *reinterpret_cast<uint2*>(dst + 32) = make_uint2(pack2(en[0], en[1]), pack2(en[2], en[3]));
      *reinterpret_cast<uint2*>(dst + 48) = make_uint2(pack2(en[4], en[5]), pack2(en[6], en[7]));
    }
    {
      bf16x8 bk = *reinterpret_cast<const bf16x8*>(qkv_img + m * QS + 64 + 16 * g);
      f32x4 p0 = __builtin_amdgcn_mfma_f32_16x16x32_bf16(aphi[0], bk, z4, 0, 0, 0);
      f32x4 p1 = __builtin_amdgcn_mfma_f32_16x16x32_bf16(aphi[1], bk, z4, 0, 0, 0);
      float ep[8], en[8];
      softmax_pm(p0, p1, ep, en);
      char* dst = pk_img + m * 128 + 8 * g;
      *reinterpret_cast<uint2*>(dst)      = make_uint2(pack2(ep[0], ep[1]), pack2(ep[2], ep[3]));
      *reinterpret_cast<uint2*>(dst + 32) = make_uint2(pack2(ep[4], ep[5]), pack2(ep[6], ep[7]));
      *reinterpret_cast<uint2*>(dst + 64) = make_uint2(pack2(en[0], en[1]), pack2(en[2], en[3]));
      *reinterpret_cast<uint2*>(dst + 96) = make_uint2(pack2(en[4], en[5]), pack2(en[6], en[7]));
    }
  }
  lds_barrier();
  STAMP(2, 2);
  {
    f32x4 c0 = {0.f, 0.f, 0.f, 0.f}, cz = c0;
    bf16x8 ones;
#pragma unroll
    for (int j = 0; j < 8; ++j) ones[j] = (short)0x3f80;
    const int q = l15 >> 2, p = lane & 3, ft = wid & 3, dt = wid >> 2;
#pragma unroll
    for (int ks = 0; ks < 8; ++ks) {
      const int r0 = ks * 32 + 8 * g + q;
      s16x4 a0 = __builtin_amdgcn_ds_read_tr16_b64_v4i16((LDSP(s16x4))(pk_img + (r0) * 128 + (16 * ft + 4 * p) * 2));
      s16x4 a1 = __builtin_amdgcn_ds_read_tr16_b64_v4i16((LDSP(s16x4))(pk_img + (r0 + 4) * 128 + (16 * ft + 4 * p) * 2));
      bf16x8 afr = __builtin_shufflevector(a0, a1, 0, 1, 2, 3, 4, 5, 6, 7);
      s16x4 b0 = __builtin_amdgcn_ds_read_tr16_b64_v4i16((LDSP(s16x4))(qkv_img + (r0) * QS + 128 + (16 * dt + 4 * p) * 2));
      s16x4 b1 = __builtin_amdgcn_ds_read_tr16_b64_v4i16((LDSP(s16x4))(qkv_img + (r0 + 4) * QS + 128 + (16 * dt + 4 * p) * 2));
      bf16x8 bf0 = __builtin_shufflevector(b0, b1, 0, 1, 2, 3, 4, 5, 6, 7);
      c0 = __builtin_amdgcn_mfma_f32_16x16x32_bf16(afr, bf0, c0, 0, 0, 0);
      cz = __builtin_amdgcn_mfma_f32_16x16x32_bf16(afr, ones, cz, 0, 0, 0);
    }
    float* kd = kvp + ((size_t)(mtile * H + head) * 64) * 32;
#pragma unroll
    for (int r = 0; r < 4; ++r) {
      const int f = 16 * ft + 4 * g + r;
      kd[f * 32 + 16 * dt + l15] = c0[r];
      if (l15 == 0 && dt == 0) zp[(mtile * H + head) * 64 + f] = cz[r];
    }
  }
  STAMP(2, 3);
}

__global__ __launch_bounds__(256) void k_attn_o(const u16* __restrict__ phq, const float* __restrict__ kvp,
                                                const float* __restrict__ zp, u16* __restrict__ o,
                                                const u16* __restrict__ wg, const u16* __restrict__ wu,
                                                const u16* __restrict__ wd) {
  if (blockIdx.x >= 256) {
    const int pb = blockIdx.x - 256;
    prefetch_range(wg, (size_t)D * F * 2, 255, pb);
    prefetch_range(wu, (size_t)D * F * 2, 255, pb);
    prefetch_range(wd, (size_t)D * F * 2, 255, pb);
    return;
  }
  __shared__ __attribute__((aligned(16))) u16 kvT[32][64 + 8];
  __shared__ float zs[64];
  const int head = blockIdx.x & 31, chunk = blockIdx.x >> 5;
  const int t = threadIdx.x, wid = t >> 6, lane = t & 63, l15 = lane & 15, g = lane >> 4;
  {
    const int f = t >> 2, d0 = (t & 3) * 8;
    float s[8];
#pragma unroll
    for (int j = 0; j < 8; ++j) s[j] = 0.f;
#pragma unroll
    for (int p = 0; p < 8; ++p) {
      const float* src = kvp + ((size_t)(p * H + head) * 64 + f) * 32 + d0;
      float4 a = *reinterpret_cast<const float4*>(src);
      float4 b2 = *reinterpret_cast<const float4*>(src + 4);
      s[0] += a.x; s[1] += a.y; s[2] += a.z; s[3] += a.w;
      s[4] += b2.x; s[5] += b2.y; s[6] += b2.z; s[7] += b2.w;
    }
#pragma unroll
    for (int j = 0; j < 8; ++j) kvT[d0 + j][f] = f2bf(s[j]);
    if (t < 64) {
      float z = 0.f;
#pragma unroll
      for (int p = 0; p < 8; ++p) z += zp[(p * H + head) * 64 + t];
      zs[t] = rbf(z);
    }
  }
  __syncthreads();
  bf16x8 af[2][2];
#pragma unroll
  for (int dt = 0; dt < 2; ++dt)
#pragma unroll
    for (int kk = 0; kk < 2; ++kk)
      af[dt][kk] = *reinterpret_cast<const bf16x8*>(&kvT[16 * dt + l15][32 * kk + 8 * g]);
  float zr[2][8];
#pragma unroll
  for (int kk = 0; kk < 2; ++kk)
#pragma unroll
    for (int j = 0; j < 8; ++j) zr[kk][j] = zs[32 * kk + 8 * g + j];
#pragma unroll
  for (int mt = 0; mt < 4; ++mt) {
    const int m = chunk * 256 + wid * 64 + mt * 16 + l15;
    const u16* src = phq + ((size_t)m * H + head) * 64 + 8 * g;
    bf16x8 b0 = *reinterpret_cast<const bf16x8*>(src);
    bf16x8 b1 = *reinterpret_cast<const bf16x8*>(src + 32);
    float den = 0.f;
#pragma unroll
    for (int j = 0; j < 8; ++j) den += bf2f((u16)b0[j]) * zr[0][j] + bf2f((u16)b1[j]) * zr[1][j];
    den += __shfl_xor(den, 16); den += __shfl_xor(den, 32);
    den = rbf(rbf(den) + 1e-6f);
    const float rden = 1.f / den;
#pragma unroll
    for (int dt = 0; dt < 2; ++dt) {
      f32x4 c = {0.f, 0.f, 0.f, 0.f};
      c = __builtin_amdgcn_mfma_f32_16x16x32_bf16(af[dt][0], b0, c, 0, 0, 0);
      c = __builtin_amdgcn_mfma_f32_16x16x32_bf16(af[dt][1], b1, c, 0, 0, 0);
      *reinterpret_cast<uint2*>(o + (size_t)m * D + head * 32 + 16 * dt + 4 * g) =
          make_uint2(pack2(rbf(c[0]) * rden, rbf(c[1]) * rden), pack2(rbf(c[2]) * rden, rbf(c[3]) * rden));
    }
  }
}

__global__ __launch_bounds__(512) void k_oproj(const u16* __restrict__ o, const u16* __restrict__ wo,
                                               const u16* __restrict__ x, const float* __restrict__ modpart,
                                               u16* __restrict__ x1) {
  extern __shared__ __attribute__((aligned(16))) char lds[];
  const int b = blockIdx.x, xcd = b & 7, idx = b >> 3;
  const int mtile = 2 * xcd + (idx & 1), ntile = idx >> 1;
  const int m0 = mtile * 128, n0 = ntile * 64;
  STAMP(4, 0);
  const int t = threadIdx.x, wid = t >> 6, lane = t & 63, l15 = lane & 15, g = lane >> 4;
  f32x4 acc[2][2];
#pragma unroll
  for (int i = 0; i < 2; ++i)
#pragma unroll
    for (int j = 0; j < 2; ++j) acc[i][j] = (f32x4){0.f, 0.f, 0.f, 0.f};
  gemm_core<128, 4, 4, 2, 6>(o, wo, D, m0, [&](int nb) { return n0 + nb * 16; }, lds, acc);
  const int wm = wid >> 1, wn = wid & 1;
  STAMP(4, 1);
#pragma unroll
  for (int nb = 0; nb < 2; ++nb) {
    const int n = n0 + (wn * 2 + nb) * 16 + 4 * g;
    float ga[4];
#pragma unroll
    for (int v = 0; v < 2; ++v) {}
    {
      float4 a = *reinterpret_cast<const float4*>(modpart + 2 * D + n);
#pragma unroll
      for (int p = 1; p < 4; ++p) {
        float4 a2 = *reinterpret_cast<const float4*>(modpart + p * 6144 + 2 * D + n);
        a.x += a2.x; a.y += a2.y; a.z += a2.z; a.w += a2.w;
      }
      ga[0] = rbf(a.x); ga[1] = rbf(a.y); ga[2] = rbf(a.z); ga[3] = rbf(a.w);
    }
#pragma unroll
    for (int mt = 0; mt < 2; ++mt) {
      const int m = m0 + wm * 32 + mt * 16 + l15;
      uint2 xv = *reinterpret_cast<const uint2*>(x + (size_t)m * D + n);
      float r0 = lo16(xv.x) + rbf(ga[0] * rbf(acc[nb][mt][0]));
      float r1 = hi16(xv.x) + rbf(ga[1] * rbf(acc[nb][mt][1]));
      float r2 = lo16(xv.y) + rbf(ga[2] * rbf(acc[nb][mt][2]));
      float r3 = hi16(xv.y) + rbf(ga[3] * rbf(acc[nb][mt][3]));
      *reinterpret_cast<uint2*>(x1 + (size_t)m * D + n) = make_uint2(pack2(r0, r1), pack2(r2, r3));
    }
  }
  STAMP(4, 2);
}

#define RLX_AGENT __ATOMIC_RELAXED, __HIP_MEMORY_SCOPE_AGENT
DI void st_agent(float* p, float v) { __hip_atomic_store(p, v, RLX_AGENT); }
DI float ld_agent(const float* p) { return __hip_atomic_load(p, RLX_AGENT); }
DI void signal(unsigned* ctr) {
  asm volatile("s_waitcnt vmcnt(0)" ::: "memory");
  __syncthreads();
  if (threadIdx.x == 0) __hip_atomic_fetch_add(ctr, 1u, RLX_AGENT);
}
DI void wait_ctr(unsigned* ctr, unsigned target) {
  if (threadIdx.x == 0) {
    for (unsigned spins = 0; spins < 4000000u; ++spins) {
      if (__hip_atomic_load(ctr, RLX_AGENT) >= target) break;
      __builtin_amdgcn_s_sleep(1);
    }
  }
  __syncthreads();
}

DI void ln_row(const u16* xr, int lane, float (&f)[16], float& mean, float& rstd) {
  uint4 v0 = *reinterpret_cast<const uint4*>(xr + lane * 8);
  uint4 v1 = *reinterpret_cast<const uint4*>(xr + 512 + lane * 8);
  f[0] = lo16(v0.x); f[1] = hi16(v0.x); f[2] = lo16(v0.y); f[3] = hi16(v0.y);
  f[4] = lo16(v0.z); f[5] = hi16(v0.z); f[6] = lo16(v0.w); f[7] = hi16(v0.w);
  f[8] = lo16(v1.x); f[9] = hi16(v1.x); f[10] = lo16(v1.y); f[11] = hi16(v1.y);
  f[12] = lo16(v1.z); f[13] = hi16(v1.z); f[14] = lo16(v1.w); f[15] = hi16(v1.w);
  float s = 0.f;
#pragma unroll
  for (int j = 0; j < 16; ++j) s += f[j];
#pragma unroll
  for (int m = 1; m < 64; m <<= 1) s += __shfl_xor(s, m);
  mean = s * (1.f / D);
  float q = 0.f;
#pragma unroll
  for (int j = 0; j < 16; ++j) { float d = f[j] - mean; q += d * d; }
#pragma unroll
  for (int m = 1; m < 64; m <<= 1) q += __shfl_xor(q, m);
  rstd = rsqrtf(q * (1.f / D) + 1e-5f);
}

__global__ __launch_bounds__(256) void k_ln2(const u16* __restrict__ x1, const float* __restrict__ modpart,
                                             const u16* __restrict__ wr, u16* __restrict__ h2,
                                             u16* __restrict__ out, float* __restrict__ r) {
  __shared__ float sc[D], sh[D], gbv[D];
  const int t = threadIdx.x, wid = t >> 6, lane = t & 63;
  uint4 xv[2][2];
#pragma unroll
  for (int q2 = 0; q2 < 2; ++q2) {
    const u16* xr = x1 + (size_t)(blockIdx.x * 8 + wid * 2 + q2) * D;
    xv[q2][0] = *reinterpret_cast<const uint4*>(xr + lane * 8);
    xv[q2][1] = *reinterpret_cast<const uint4*>(xr + 512 + lane * 8);
  }
  uint4 w0 = *reinterpret_cast<const uint4*>(wr + lane * 8);
  uint4 w1 = *reinterpret_cast<const uint4*>(wr + 512 + lane * 8);
  {
    const int c4 = t * 4;
    float4 a = *reinterpret_cast<const float4*>(modpart + 3 * D + c4);
    float4 bb = *reinterpret_cast<const float4*>(modpart + 4 * D + c4);
    float4 c = *reinterpret_cast<const float4*>(modpart + 5 * D + c4);
#pragma unroll
    for (int p = 1; p < 4; ++p) {
      float4 a2 = *reinterpret_cast<const float4*>(modpart + p * 6144 + 3 * D + c4);
      float4 b2 = *reinterpret_cast<const float4*>(modpart + p * 6144 + 4 * D + c4);
      float4 c2 = *reinterpret_cast<const float4*>(modpart + p * 6144 + 5 * D + c4);
      a.x += a2.x; a.y += a2.y; a.z += a2.z; a.w += a2.w;
      bb.x += b2.x; bb.y += b2.y; bb.z += b2.z; bb.w += b2.w;
      c.x += c2.x; c.y += c2.y; c.z += c2.z; c.w += c2.w;
    }
    sh[c4] = rbf(a.x); sh[c4 + 1] = rbf(a.y); sh[c4 + 2] = rbf(a.z); sh[c4 + 3] = rbf(a.w);
    sc[c4] = rbf(1.f + rbf(bb.x)); sc[c4 + 1] = rbf(1.f + rbf(bb.y));
    sc[c4 + 2] = rbf(1.f + rbf(bb.z)); sc[c4 + 3] = rbf(1.f + rbf(bb.w));
    gbv[c4] = rbf(c.x); gbv[c4 + 1] = rbf(c.y); gbv[c4 + 2] = rbf(c.z); gbv[c4 + 3] = rbf(c.w);
  }
  __syncthreads();
  float wf[16];
  wf[0] = lo16(w0.x); wf[1] = hi16(w0.x); wf[2] = lo16(w0.y); wf[3] = hi16(w0.y);
  wf[4] = lo16(w0.z); wf[5] = hi16(w0.z); wf[6] = lo16(w0.w); wf[7] = hi16(w0.w);
  wf[8] = lo16(w1.x); wf[9] = hi16(w1.x); wf[10] = lo16(w1.y); wf[11] = hi16(w1.y);
  wf[12] = lo16(w1.z); wf[13] = hi16(w1.z); wf[14] = lo16(w1.w); wf[15] = hi16(w1.w);
#pragma unroll
  for (int q2 = 0; q2 < 2; ++q2) {
    const int row = blockIdx.x * 8 + wid * 2 + q2;
    const uint4 v0 = xv[q2][0], v1 = xv[q2][1];
    float f[16];
    f[0] = lo16(v0.x); f[1] = hi16(v0.x); f[2] = lo16(v0.y); f[3] = hi16(v0.y);
    f[4] = lo16(v0.z); f[5] = hi16(v0.z); f[6] = lo16(v0.w); f[7] = hi16(v0.w);
    f[8] = lo16(v1.x); f[9] = hi16(v1.x); f[10] = lo16(v1.y); f[11] = hi16(v1.y);
    f[12] = lo16(v1.z); f[13] = hi16(v1.z); f[14] = lo16(v1.w); f[15] = hi16(v1.w);
    float s = 0.f;
#pragma unroll
    for (int j = 0; j < 16; ++j) s += f[j];
#pragma unroll
    for (int m = 1; m < 64; m <<= 1) s += __shfl_xor(s, m);
    const float mean = s * (1.f / D);
    float q = 0.f;
#pragma unroll
    for (int j = 0; j < 16; ++j) { float d = f[j] - mean; q += d * d; }
#pragma unroll
    for (int m = 1; m < 64; m <<= 1) q += __shfl_xor(q, m);
    const float rstd = rsqrtf(q * (1.f / D) + 1e-5f);
    unsigned ho[8], oo[8];
    float dot = 0.f;
#pragma unroll
    for (int j = 0; j < 16; j += 2) {
      const int c0 = (j < 8) ? lane * 8 + j : 512 + lane * 8 + (j - 8);
      float a = rbf(rbf((f[j] - mean) * rstd) * sc[c0] + sh[c0]);
      float bq = rbf(rbf((f[j + 1] - mean) * rstd) * sc[c0 + 1] + sh[c0 + 1]);
      dot += a * wf[j] + bq * wf[j + 1];
      ho[j >> 1] = pack2(a, bq);
      oo[j >> 1] = pack2(f[j] + rbf(gbv[c0] * a), f[j + 1] + rbf(gbv[c0 + 1] * bq));
    }
#pragma unroll
    for (int m = 1; m < 64; m <<= 1) dot += __shfl_xor(dot, m);
    u16* hr = h2 + (size_t)row * D;
    *reinterpret_cast<uint4*>(hr + lane * 8) = make_uint4(ho[0], ho[1], ho[2], ho[3]);
    *reinterpret_cast<uint4*>(hr + 512 + lane * 8) = make_uint4(ho[4], ho[5], ho[6], ho[7]);
    u16* orow = out + (size_t)row * D;
    *reinterpret_cast<uint4*>(orow + lane * 8) = make_uint4(oo[0], oo[1], oo[2], oo[3]);
    *reinterpret_cast<uint4*>(orow + 512 + lane * 8) = make_uint4(oo[4], oo[5], oo[6], oo[7]);
    if (lane == 0) r[row] = rbf(dot);
  }
}

__global__ __launch_bounds__(256) void k_moe(const float* __restrict__ r, const u16* __restrict__ h2,
                                             const u16* __restrict__ x1, const float* __restrict__ modpart,
                                             const u16* __restrict__ wg, const u16* __restrict__ wu,
                                             const u16* __restrict__ wd, u16* __restrict__ out,
                                             float* __restrict__ gup, float* __restrict__ dpart,
                                             unsigned* __restrict__ ctr) {
  __shared__ float red[32][65];
  __shared__ float hs[2][256];
  __shared__ float sm_v[8]; __shared__ int sm_i[8];
  __shared__ int s_last;
  const int b = blockIdx.x, t = threadIdx.x, wid = t >> 6, lane = t & 63;
  const int cq = t & 7, rr = t >> 3;
  const int gcc = b % 56, gks = b / 56;
  const int dcc = b & 15, dks = b >> 4;
  STAMP(7, 0);
  float rv8[8];
#pragma unroll
  for (int i = 0; i < 8; ++i) rv8[i] = r[t + i * 256];
  uint4 wa[8], wb[8];
#pragma unroll
  for (int i = 0; i < 8; ++i) {
    const int k = gks * 256 + i * 32 + rr;
    wa[i] = *reinterpret_cast<const uint4*>(wg + (size_t)k * F + gcc * 64 + cq * 8);
    wb[i] = *reinterpret_cast<const uint4*>(wu + (size_t)k * F + gcc * 64 + cq * 8);
  }
  int i0, i1; float r0, r1;
  {
    float b0 = -INFINITY, b1 = -INFINITY; int j0 = 0x7fffffff, j1 = 0x7fffffff;
#pragma unroll
    for (int i = 0; i < 8; ++i) {
      const int idx = t + i * 256;
      const float v = rv8[i];
      if (v > b0 || (v == b0 && idx < j0)) { b1 = b0; j1 = j0; b0 = v; j0 = idx; }
      else if (v > b1 || (v == b1 && idx < j1)) { b1 = v; j1 = idx; }
    }
#pragma unroll
    for (int m = 1; m < 64; m <<= 1) {
      float o0 = __shfl_xor(b0, m), o1 = __shfl_xor(b1, m);
      int p0 = __shfl_xor(j0, m), p1 = __shfl_xor(j1, m);
      if (o0 > b0 || (o0 == b0 && p0 < j0)) {
        if (b0 > o1 || (b0 == o1 && j0 < p1)) { b1 = b0; j1 = j0; } else { b1 = o1; j1 = p1; }
        b0 = o0; j0 = p0;
      } else {
        if (o0 > b1 || (o0 == b1 && p0 < j1)) { b1 = o0; j1 = p0; }
      }
    }
    if (lane == 0) { sm_v[wid * 2] = b0; sm_v[wid * 2 + 1] = b1; sm_i[wid * 2] = j0; sm_i[wid * 2 + 1] = j1; }
    __syncthreads();
    b0 = sm_v[0]; b1 = sm_v[1]; j0 = sm_i[0]; j1 = sm_i[1];
#pragma unroll
    for (int w = 1; w < 4; ++w) {
      float o0 = sm_v[w * 2], o1 = sm_v[w * 2 + 1];
      int p0 = sm_i[w * 2], p1 = sm_i[w * 2 + 1];
      if (o0 > b0 || (o0 == b0 && p0 < j0)) {
        if (b0 > o1 || (b0 == o1 && j0 < p1)) { b1 = b0; j1 = j0; } else { b1 = o1; j1 = p1; }
        b0 = o0; j0 = p0;
      } else {
        if (o0 > b1 || (o0 == b1 && p0 < j1)) { b1 = o0; j1 = p0; }
      }
    }
    i0 = j0; i1 = j1; r0 = b0; r1 = b1;
  }
  STAMP(7, 1);
  float hv0[8], hv1[8];
#pragma unroll
  for (int i = 0; i < 8; ++i) {
    const int k = gks * 256 + i * 32 + rr;
    hv0[i] = bf2f(h2[(size_t)i0 * D + k]); hv1[i] = bf2f(h2[(size_t)i1 * D + k]);
  }
  {
    float acc[4][8];
#pragma unroll
    for (int a = 0; a < 4; ++a)
#pragma unroll
      for (int j = 0; j < 8; ++j) acc[a][j] = 0.f;
#pragma unroll
    for (int i = 0; i < 8; ++i) {
      const float h0 = hv0[i], h1v = hv1[i];
      const unsigned wv[4] = {wa[i].x, wa[i].y, wa[i].z, wa[i].w};
      const unsigned uv[4] = {wb[i].x, wb[i].y, wb[i].z, wb[i].w};
#pragma unroll
      for (int j = 0; j < 4; ++j) {
        acc[0][2 * j] += h0 * lo16(wv[j]);  acc[0][2 * j + 1] += h0 * hi16(wv[j]);
        acc[1][2 * j] += h1v * lo16(wv[j]); acc[1][2 * j + 1] += h1v * hi16(wv[j]);
        acc[2][2 * j] += h0 * lo16(uv[j]);  acc[2][2 * j + 1] += h0 * hi16(uv[j]);
        acc[3][2 * j] += h1v * lo16(uv[j]); acc[3][2 * j + 1] += h1v * hi16(uv[j]);
      }
    }
#pragma unroll
    for (int a = 0; a < 4; ++a) {
      __syncthreads();
#pragma unroll
      for (int j = 0; j < 8; ++j) red[rr][cq * 8 + j] = acc[a][j];
      __syncthreads();
      if (t < 64) {
        float sum = 0.f;
#pragma unroll
        for (int i = 0; i < 32; ++i) sum += red[i][t];
        st_agent(gup + (size_t)(gks * 4 + a) * F + gcc * 64 + t, sum);
      }
    }
  }
  STAMP(7, 2);
  signal(ctr + 16 + (gcc >> 2));
  uint4 wdv[8];
#pragma unroll
  for (int i = 0; i < 8; ++i) {
    const int k = dks * 256 + i * 32 + rr;
    wdv[i] = *reinterpret_cast<const uint4*>(wd + (size_t)k * D + dcc * 64 + cq * 8);
  }
  wait_ctr(ctr + 16 + dks, 16);
  STAMP(7, 3);
  {
    const int k = dks * 256 + t;
#pragma unroll
    for (int tok = 0; tok < 2; ++tok) {
      float gsum = 0.f, usum = 0.f;
#pragma unroll
      for (int p = 0; p < 4; ++p) {
        gsum += ld_agent(gup + (size_t)(p * 4 + tok) * F + k);
        usum += ld_agent(gup + (size_t)(p * 4 + 2 + tok) * F + k);
      }
      gsum = rbf(gsum); usum = rbf(usum);
      const float sg = rbf(gsum / (1.f + __expf(-gsum)));
      hs[tok][t] = rbf(sg * usum);
    }
  }
  __syncthreads();
  {
    float ac2[2][8];
#pragma unroll
    for (int a = 0; a < 2; ++a)
#pragma unroll
      for (int j = 0; j < 8; ++j) ac2[a][j] = 0.f;
#pragma unroll
    for (int i = 0; i < 8; ++i) {
      const int kl = i * 32 + rr;
      const float h0 = hs[0][kl], h1v = hs[1][kl];
      const unsigned wv[4] = {wdv[i].x, wdv[i].y, wdv[i].z, wdv[i].w};
#pragma unroll
      for (int j = 0; j < 4; ++j) {
        ac2[0][2 * j] += h0 * lo16(wv[j]);  ac2[0][2 * j + 1] += h0 * hi16(wv[j]);
        ac2[1][2 * j] += h1v * lo16(wv[j]); ac2[1][2 * j + 1] += h1v * hi16(wv[j]);
      }
    }
#pragma unroll
    for (int a = 0; a < 2; ++a) {
      __syncthreads();
#pragma unroll
      for (int j = 0; j < 8; ++j) red[rr][cq * 8 + j] = ac2[a][j];
      __syncthreads();
      if (t < 64) {
        float sum = 0.f;
#pragma unroll
        for (int i = 0; i < 32; ++i) sum += red[i][t];
        st_agent(dpart + (size_t)(dks * 2 + a) * D + dcc * 64 + t, sum);
      }
    }
  }
  STAMP(7, 4);
  asm volatile("s_waitcnt vmcnt(0)" ::: "memory");
  __syncthreads();
  if (t == 0) s_last = (__hip_atomic_fetch_add(ctr + 32 + dcc, 1u, RLX_AGENT) == 13u);
  __syncthreads();
  if (s_last && t < 128) {
    const int tok = t >> 6, n = dcc * 64 + (t & 63);
    const int row = tok ? i1 : i0;
    const float rv = tok ? r1 : r0;
    float y = 0.f;
#pragma unroll
    for (int p = 0; p < 14; ++p) y += ld_agent(dpart + (size_t)(p * 2 + tok) * D + n);
    const float sig = rbf(1.f / (1.f + __expf(-rv)));
    y = rbf(rbf(y) * sig);
    float gb = 0.f;
#pragma unroll
    for (int p = 0; p < 4; ++p) gb += modpart[p * 6144 + 5 * D + n];
    gb = rbf(gb);
    const float hv = bf2f(h2[(size_t)row * D + n]);
    const float xv = bf2f(x1[(size_t)row * D + n]);
    out[(size_t)row * D + n] = f2bf(xv + rbf(gb * rbf(hv + y)));
  }
}

extern "C" void kernel_launch(void* const* d_in, const int* in_sizes, int n_in, void* d_out, int out_size,
                              void* d_ws, size_t ws_size, hipStream_t stream) {
  const u16* x    = (const u16*)d_in[0];
  const u16* wqkv = (const u16*)d_in[1];
  const u16* wphi = (const u16*)d_in[2];
  const u16* wo   = (const u16*)d_in[3];
  const u16* w_r  = (const u16*)d_in[5];
  const u16* wg   = (const u16*)d_in[6];
  const u16* wu   = (const u16*)d_in[7];
  const u16* wd   = (const u16*)d_in[8];
  const u16* c    = (const u16*)d_in[9];
  const u16* wmod = (const u16*)d_in[10];
  u16* out = (u16*)d_out;

  char* ws = (char*)d_ws;
  size_t off = 0;
  auto alloc = [&](size_t bytes) { char* p = ws + off; off += (bytes + 255) & ~(size_t)255; return p; };
  float* modpart = (float*)alloc(4 * 6144 * 4);
  u16* h1  = (u16*)alloc((size_t)S * D * 2);
  u16* phq = (u16*)alloc((size_t)S * H * 64 * 2);
  float* kvp = (float*)alloc((size_t)8 * H * 64 * 32 * 4);
  float* zp  = (float*)alloc((size_t)8 * H * 64 * 4);
  u16* o   = (u16*)alloc((size_t)S * D * 2);
  u16* x1  = (u16*)alloc((size_t)S * D * 2);
  u16* h2  = (u16*)alloc((size_t)S * D * 2);
  float* r = (float*)alloc(S * 4);
  float* gup = (float*)alloc((size_t)16 * F * 4);
  float* dpart = (float*)alloc((size_t)28 * D * 4);
  unsigned* ctr = (unsigned*)alloc(256);

  k_mod<<<384, 256, 0, stream>>>(c, wmod, modpart, ctr);
  k_ln1<<<256, 256, 0, stream>>>(x, modpart, h1);
  constexpr int LDS_QKV = 3 * (256 * 128 + 64 * 192);
  constexpr int LDS_OP  = 6 * (128 * 128 + 64 * 128);
  hipFuncSetAttribute((const void*)k_qkv, hipFuncAttributeMaxDynamicSharedMemorySize, LDS_QKV);
  hipFuncSetAttribute((const void*)k_oproj, hipFuncAttributeMaxDynamicSharedMemorySize, LDS_OP);
  k_qkv<<<256, 512, LDS_QKV, stream>>>(h1, wqkv, wphi, phq, kvp, zp);
  k_attn_o<<<256 + PFB, 256, 0, stream>>>(phq, kvp, zp, o, wg, wu, wd);
  k_oproj<<<256, 512, LDS_OP, stream>>>(o, wo, x, modpart, x1);
  k_ln2<<<256, 256, 0, stream>>>(x1, modpart, w_r, h2, out, r);
  k_moe<<<224, 256, 0, stream>>>(r, h2, x1, modpart, wg, wu, wd, out, gup, dpart, ctr);
}
```
